```python
import torch

def _softmax(x, axis=-1):
    return torch.softmax(x, dim=axis)
def _gelu(x):
    return torch.nn.functional.gelu(x, approximate="tanh")
def _silu(x):
    return torch.nn.functional.silu(x)
def _sigmoid(x):
    return torch.sigmoid(x)
def _layernorm(x, axis=-1, eps=1e-5):
    m = x.mean(dim=axis, keepdim=True)
    v = x.var(dim=axis, keepdim=True, unbiased=False)
    return (x - m) / torch.sqrt(v + eps)
def _scatter_add(tgt, idx, src, axis=0):
    return tgt.clone().index_add_(axis, idx, src)
def _segment_sum(src, seg, n):
    out = torch.zeros((n,) + src.shape[1:], dtype=src.dtype)
    return out.index_add_(0, seg, src)
def _pad(x, pw):
    flat = tuple(v for pair in reversed(pw) for v in pair)
    return torch.nn.functional.pad(x, flat)

kernel_name = 'parallel_sum__grouped_kv_mqa__glu_gated_geglu__prompt'

def setup_inputs():

    _w = (lambda a: (lambda t: t.to(torch.bfloat16) if t.is_floating_point() else t)(torch.as_tensor(a)))
    def _A_setup():
        B, S, D, H = 1, 2048, 512, 32
        Dh = D // H
        return {
            "x": _w((lambda *s: torch.randn(*s, dtype=torch.bfloat16) * 0.05)(B, S, D)),
            "wq": _w((lambda *s: torch.randn(*s, dtype=torch.bfloat16) * 0.05)(D, H * Dh)),
            "wk": _w((lambda *s: torch.randn(*s, dtype=torch.bfloat16) * 0.05)(D, Dh)),
            "wv": _w((lambda *s: torch.randn(*s, dtype=torch.bfloat16) * 0.05)(D, Dh)),
            "wo": _w((lambda *s: torch.randn(*s, dtype=torch.bfloat16) * 0.05)(H * Dh, D)),
        }
    _A_in = _A_setup()

    def _B_setup():
        B, S, Dm, Df = 1, 2048, 512, 1792
        return {
            "x": _w((lambda *s: torch.randn(*s, dtype=torch.bfloat16) * 0.05)(B, S, Dm)),
            "w_gate": _w((lambda *s: torch.randn(*s, dtype=torch.bfloat16) * 0.4)(Dm, Df) * (Dm ** -0.5)),
            "w_up": _w((lambda *s: torch.randn(*s, dtype=torch.bfloat16) * 0.4)(Dm, Df) * (Dm ** -0.5)),
            "w_down": _w((lambda *s: torch.randn(*s, dtype=torch.bfloat16) * 0.4)(Df, Dm) * (Df ** -0.5)),
        }
    _B_in = _B_setup()

    return {
        "A_x": _A_in["x"],
        "A_wq": _A_in["wq"],
        "A_wk": _A_in["wk"],
        "A_wv": _A_in["wv"],
        "A_wo": _A_in["wo"],
        "B_x": _B_in["x"],
        "B_w_gate": _B_in["w_gate"],
        "B_w_up": _B_in["w_up"],
        "B_w_down": _B_in["w_down"],
    }

def reference(A_x, A_wq, A_wk, A_wv, A_wo, B_x, B_w_gate, B_w_up, B_w_down):
    def _A_ref(x, wq, wk, wv, wo):
        B, S, D = x.shape
        H = 32
        Dh = D // H
        q = (lambda x, *d: x.permute(*d))((x @ wq).reshape(B, S, H, Dh), 0, 2, 1, 3)
        k = x @ wk
        v = x @ wv
        scores = torch.einsum("bhqd,bkd->bhqk", q, k) * (Dh ** -0.5)
        p = _softmax(scores, axis=-1)
        o = torch.einsum("bhqk,bkd->bhqd", p, v)
        return (lambda x, *d: x.permute(*d))(o, 0, 2, 1, 3).reshape(B, S, H * Dh) @ wo

    def _B_ref(x, w_gate, w_up, w_down):
        g = torch.einsum("bsd,df->bsf", x, w_gate)
        u = torch.einsum("bsd,df->bsf", x, w_up)
        h = _gelu(g) * u
        return torch.einsum("bsf,fd->bsd", h, w_down)

    x = A_x
    xn = _layernorm(x, axis=-1)
    return x + _A_ref(xn, A_wq, A_wk, A_wv, A_wo) + _B_ref(xn, B_w_gate, B_w_up, B_w_down)
```

```cpp
#include <hip/hip_runtime.h>
#include <hip/hip_bf16.h>
#include <stdint.h>

typedef __bf16 bf16x8 __attribute__((ext_vector_type(8)));
typedef __bf16 bf16x2 __attribute__((ext_vector_type(2)));
typedef short s16x4 __attribute__((ext_vector_type(4)));
typedef float f32x16 __attribute__((ext_vector_type(16)));
typedef float f32x4 __attribute__((ext_vector_type(4)));
typedef unsigned u32x4 __attribute__((ext_vector_type(4)));
typedef unsigned u32x2 __attribute__((ext_vector_type(2)));
typedef unsigned short u16;
#define LAS __attribute__((address_space(3)))
#define DI __device__ __forceinline__
#define MFMA(a, b, c) __builtin_amdgcn_mfma_f32_32x32x16_bf16((a), (b), (c), 0, 0, 0)

constexpr int SEQ = 2048, DM = 512, NH = 32, DF = 1792;
constexpr int NKS = 144;
constexpr size_t WS_KF = 0;
constexpr size_t WS_VF = 65536;
constexpr size_t KV_BYTES = 65536 + 64 * 1088;
constexpr size_t WS_QF = 262144;
constexpr size_t WS_ACT = WS_QF + (size_t)64 * 32 * 1024;
constexpr size_t WS_W2F = WS_ACT + (size_t)64 * NKS * 1024;
constexpr size_t WS_FLAG = WS_W2F + (size_t)16 * NKS * 1024;
constexpr size_t WS_PART = WS_FLAG + 16384;

#define STAMP(kern, i)
DI unsigned cvtpk(float lo, float hi) {
    bf16x2 r = {(__bf16)lo, (__bf16)hi};
    return __builtin_bit_cast(unsigned, r);
}
DI float bf_round(float v) { return (float)(__bf16)v; }
DI float swap_add(float v) {
    auto rr = __builtin_amdgcn_permlane32_swap(__float_as_uint(v), __float_as_uint(v), false, false);
    return __uint_as_float(rr[0]) + __uint_as_float(rr[1]);
}
DI float swap_max(float v) {
    auto rr = __builtin_amdgcn_permlane32_swap(__float_as_uint(v), __float_as_uint(v), false, false);
    return __builtin_fmaxf(__uint_as_float(rr[0]), __uint_as_float(rr[1]));
}
DI void glds16(const void* g, unsigned lds_base) {
    unsigned sv;
    asm volatile("s_mov_b32 %0, m0\n\ts_mov_b32 m0, %2\n\ts_nop 0\n\tglobal_load_lds_dwordx4 %1, off\n\ts_mov_b32 m0, %0"
                 : "=&s"(sv) : "v"(g), "s"(lds_base) : "memory");
}
DI void glds16s(const void* sbase, unsigned voff, unsigned lds_base) {
    unsigned sv;
    asm volatile("s_mov_b32 %0, m0\n\ts_mov_b32 m0, %3\n\ts_nop 0\n\tglobal_load_lds_dwordx4 %1, %2\n\ts_mov_b32 m0, %0"
                 : "=&s"(sv) : "v"(voff), "s"(sbase), "s"(lds_base) : "memory");
}
#define WAIT_BAR(N) asm volatile("s_waitcnt vmcnt(" #N ") lgkmcnt(0)\n\ts_barrier" ::: "memory")
DI bf16x8 lds_b128(LAS const char* p) { return *(LAS const bf16x8*)p; }
DI bf16x8 lds_tr2(LAS const char* p) {
    s16x4 lo = __builtin_amdgcn_ds_read_tr16_b64_v4i16((LAS s16x4*)p);
    s16x4 hi = __builtin_amdgcn_ds_read_tr16_b64_v4i16((LAS s16x4*)(p + 512));
    return __builtin_bit_cast(bf16x8, __builtin_shufflevector(lo, hi, 0, 1, 2, 3, 4, 5, 6, 7));
}
DI bf16x8 lds_tr2kv(LAS const char* p) {
    s16x4 lo = __builtin_amdgcn_ds_read_tr16_b64_v4i16((LAS s16x4*)p);
    s16x4 hi = __builtin_amdgcn_ds_read_tr16_b64_v4i16((LAS s16x4*)(p + 128));
    return __builtin_bit_cast(bf16x8, __builtin_shufflevector(lo, hi, 0, 1, 2, 3, 4, 5, 6, 7));
}
DI float dot8(bf16x8 a, bf16x8 b, float acc) {
    acc = __builtin_amdgcn_fdot2_f32_bf16(__builtin_shufflevector(a, a, 0, 1), __builtin_shufflevector(b, b, 0, 1), acc, false);
    acc = __builtin_amdgcn_fdot2_f32_bf16(__builtin_shufflevector(a, a, 2, 3), __builtin_shufflevector(b, b, 2, 3), acc, false);
    acc = __builtin_amdgcn_fdot2_f32_bf16(__builtin_shufflevector(a, a, 4, 5), __builtin_shufflevector(b, b, 4, 5), acc, false);
    acc = __builtin_amdgcn_fdot2_f32_bf16(__builtin_shufflevector(a, a, 6, 7), __builtin_shufflevector(b, b, 6, 7), acc, false);
    return acc;
}
DI float add_f(float a, float b) { float r; asm("v_add_f32 %0, %1, %2" : "=v"(r) : "v"(a), "v"(b)); return r; }
DI float fma_sq(float a, float c) { float r; asm("v_fma_f32 %0, %1, %1, %2" : "=v"(r) : "v"(a), "v"(c)); return r; }
DI void sum_dw(bf16x8 f, int i, float& s0, float& s1) {
    const unsigned d = __builtin_bit_cast(u32x4, f)[i];
    s0 = add_f(s0, __uint_as_float(d & 0xffff0000u));
    s1 = add_f(s1, __uint_as_float(d << 16));
}
DI void sq_dw(bf16x8 f, int i, float& s0, float& s1, float& q0, float& q1) {
    const unsigned d = __builtin_bit_cast(u32x4, f)[i];
    const float hi = __uint_as_float(d & 0xffff0000u), lo = __uint_as_float(d << 16);
    s0 = add_f(s0, hi); s1 = add_f(s1, lo);
    q0 = fma_sq(hi, q0); q1 = fma_sq(lo, q1);
}
#define PIN(x) asm volatile("" : "+v"(x))
DI u32x4 pack8(const f32x16& v, int o, float s) {
    u32x4 r;
#pragma unroll
    for (int i = 0; i < 4; ++i) r[i] = cvtpk(v[o + 2 * i] * s, v[o + 2 * i + 1] * s);
    return r;
}

constexpr int K1_STAGE = 49152, K1_KV = 3 * K1_STAGE;
template <int TYPE>
DI void k1_body(const u16* __restrict__ x, const u16* __restrict__ wA, const u16* __restrict__ wB, const u16* __restrict__ wkv,
                char* __restrict__ ws, char* smem, const int rt, const int cg, const int w, const int lane) {
    constexpr int ldw = TYPE == 0 ? DF : DM;
    const int wr = w & 1, wn = w >> 1;
    const int r = lane & 31, h = lane >> 5;
    const unsigned lds0 = (unsigned)(uintptr_t)smem;
    LAS const char* L = (LAS const char*)smem;

    const int prow = lane >> 3, pslot = lane & 7;
    const unsigned xo0 = (unsigned)((16 * w + prow) * DM * 2 + ((pslot ^ (((16 * w + prow) >> 1) & 7)) * 16));
    const unsigned xo1 = (unsigned)((16 * w + 8 + prow) * DM * 2 + ((pslot ^ (((16 * w + 8 + prow) >> 1) & 7)) * 16));
    const unsigned wo0 = (unsigned)((8 * w + prow) * ldw * 2 + ((pslot ^ (((prow >> 1) & 1) << 2)) * 16));
    const unsigned wo1 = wo0 + 128;
    const char* xb = (const char*)(x + (size_t)rt * 128 * DM);
    if (TYPE != 0) {
        glds16s((const char*)wkv + w * 2048, lane * 16, lds0 + K1_KV + w * 2048);
        glds16s((const char*)wkv + w * 2048 + 1024, lane * 16, lds0 + K1_KV + w * 2048 + 1024);
    }
#define K1_DMA(s, i)                                                                                      \
    do {                                                                                                  \
        if ((s) < 8) {                                                                                    \
            const unsigned sb_ = lds0 + ((s) % 3) * K1_STAGE;                                             \
            const size_t ko_ = (size_t)(s) * (64 * ldw * 2);                                              \
            if ((i) == 0) glds16s(xb + (s) * 128, xo0, sb_ + w * 2048);                                   \
            if ((i) == 1) glds16s(xb + (s) * 128, xo1, sb_ + w * 2048 + 1024);                            \
            if ((i) == 2) glds16s((const char*)wA + ko_, wo0, sb_ + 16384 + w * 4096);                    \
            if ((i) == 3) glds16s((const char*)wA + ko_, wo1, sb_ + 16384 + w * 4096 + 1024);             \
            if ((i) == 4) glds16s((const char*)wB + ko_, wo0, sb_ + 16384 + w * 4096 + 2048);             \
            if ((i) == 5) glds16s((const char*)wB + ko_, wo1, sb_ + 16384 + w * 4096 + 3072);             \
            __builtin_amdgcn_sched_barrier(0);                                                            \
        }                                                                                                 \
    } while (0)
#pragma unroll
    for (int i = 0; i < 6; ++i) K1_DMA(0, i);
#pragma unroll
    for (int i = 0; i < 6; ++i) K1_DMA(1, i);

    const int par = wn & 1;
    const int ga = 2 * wr + (wn >> 1), gb = 2 * wr + 1 - (wn >> 1);
    const int fx = ((r >> 1) & 7) ^ (par << 1);
    const int blk = (lane >> 4) & 1, q4 = (lane & 15) >> 2, p4 = lane & 3;
    const int wlane = 16384 + h * 4096 + (wn >> 1) * 1024 + q4 * 128 +
                      ((((wn & 1) * 4 + blk * 2 + (p4 >> 1)) ^ (((q4 >> 1) & 1) << 2)) * 16) + (p4 & 1) * 8;
    const int wc_off = wlane + wr * 2048, wd_off = wlane + (1 - wr) * 2048;
    const int kvlane = K1_KV + (8 * h + q4) * 32 + p4 * 8;
    const bool kvw = (wn == 0) || (wn == 3);
    bf16x8 sel;
    {
        const unsigned o1 = ((lane & 15) == blk) ? 0x3F803F80u : 0u;
        sel = __builtin_bit_cast(bf16x8, (u32x4){o1, o1, o1, o1});
    }

    f32x16 acc_ca = {}, acc_cb = {}, acc_da = {}, acc_db = {}, acc_kv = {};
    f32x4 acc_cs = {}, acc_ck = {};
    float s_sum = 0.f, s_sq = 0.f, s_sum2 = 0.f, s_sq2 = 0.f;
    bf16x8 xa[2], xbf[2], wc[2], wdd[2], kf[2];
#define K1_READ(set, s, j)                                                            \
    do {                                                                              \
        LAS const char* sb_ = L + ((s) % 3) * K1_STAGE;                               \
        const int xo_ = r * 128 + (((2 * (j) + h) ^ fx) * 16);                        \
        const int ks_ = (j) + (((j) & 1) ? -par : par);                               \
        wc[set] = lds_tr2(sb_ + wc_off + ks_ * 8192);                                 \
        xa[set] = lds_b128(sb_ + ga * 4096 + xo_);                                    \
        xbf[set] = lds_b128(sb_ + gb * 4096 + xo_);                                   \
        wdd[set] = lds_tr2(sb_ + wd_off + ks_ * 8192);                                \
        if (TYPE != 0 && kvw) kf[set] = lds_tr2kv(L + kvlane + (s) * 2048 + ks_ * 512); \
        __builtin_amdgcn_sched_barrier(0);                                            \
    } while (0)
#define K1_MMA(set, j, ds, d0, d1)                                                    \
    do {                                                                              \
        acc_ca = MFMA(wc[set], xa[set], acc_ca);                                      \
        if (((j) & 1) == 0) sq_dw(xa[set], 0, s_sum, s_sum2, s_sq, s_sq2);            \
        __builtin_amdgcn_sched_barrier(0);                                            \
        if ((d0) >= 0) K1_DMA(ds, d0);                                                \
        acc_cb = MFMA(wc[set], xbf[set], acc_cb);                                     \
        if (((j) & 1) == 0) sq_dw(xa[set], 1, s_sum, s_sum2, s_sq, s_sq2);            \
        __builtin_amdgcn_sched_barrier(0);                                            \
        acc_cs = __builtin_amdgcn_mfma_f32_16x16x32_bf16(sel, wc[set], acc_cs, 0, 0, 0); \
        acc_da = MFMA(wdd[set], xa[set], acc_da);                                     \
        if (((j) & 1) == 0) sq_dw(xa[set], 2, s_sum, s_sum2, s_sq, s_sq2);            \
        __builtin_amdgcn_sched_barrier(0);                                            \
        if ((d1) >= 0) K1_DMA(ds, d1);                                                \
        acc_db = MFMA(wdd[set], xbf[set], acc_db);                                    \
        if (((j) & 1) == 0) sq_dw(xa[set], 3, s_sum, s_sum2, s_sq, s_sq2);            \
        if (TYPE != 0 && kvw) {                                                       \
            __builtin_amdgcn_sched_barrier(0);                                        \
            if (TYPE == 1) acc_kv = MFMA(kf[set], xa[set], acc_kv); else acc_kv = MFMA(xa[set], kf[set], acc_kv); \
            acc_ck = __builtin_amdgcn_mfma_f32_16x16x32_bf16(sel, kf[set], acc_ck, 0, 0, 0); \
        }                                                                             \
        __builtin_amdgcn_sched_barrier(0);                                            \
    } while (0)
    STAMP(0, 1);
    WAIT_BAR(6);
    STAMP(0, 2);
    K1_READ(0, 0, 0);
    K1_DMA(2, 0); K1_DMA(2, 1);
#pragma unroll
    for (int s = 0; s < 8; ++s) {
        K1_READ(1, s, 1); K1_MMA(0, 0, s + 2, 2, 3);
        K1_READ(0, s, 2); K1_MMA(1, 1, s + 2, 4, -1);
        K1_READ(1, s, 3); K1_MMA(0, 2, s + 2, 5, -1);
        if (s < 7) {
            if (s < 6) WAIT_BAR(6); else WAIT_BAR(0);
            K1_READ(0, s + 1, 0);
        }
        K1_MMA(1, 3, s + 3, 0, 1);
    }
#undef K1_READ
#undef K1_MMA
#undef K1_DMA
    STAMP(0, 3);
    s_sum = swap_add(s_sum + s_sum2); s_sq = swap_add(s_sq + s_sq2);
    float* st_sum = (float*)smem;
    float* st_sq = st_sum + 256;
    float* st_cs = st_sum + 512;
    float* st_ck = st_sum + 768;
    if (h == 0) { st_sum[par * 128 + ga * 32 + r] = s_sum; st_sq[par * 128 + ga * 32 + r] = s_sq; }
    if (lane < 16) {
        st_cs[wr * 128 + wn * 32 + lane] = acc_cs[0]; st_cs[wr * 128 + wn * 32 + 16 + lane] = acc_cs[1];
        if (TYPE != 0) st_ck[w * 16 + lane] = acc_ck[0];
    }
    __syncthreads();
    const float s_cs = st_cs[wr * 128 + wn * 32 + r];
    const float s_kv = TYPE != 0 ? st_ck[w * 16 + (lane & 15)] : 0.f;
    auto rowstat = [&](int row, float& mu, float& rs) {
        const float mean = (st_sum[row] + st_sum[128 + row]) * (1.0f / DM);
        const float var = __builtin_fmaxf((st_sq[row] + st_sq[128 + row]) * (1.0f / DM) - mean * mean, 0.f);
        const float sd = bf_round(__builtin_amdgcn_sqrtf(bf_round(bf_round(var) + 1e-5f)));
        mu = bf_round(mean); rs = __builtin_amdgcn_rcpf(sd);
    };
    float mu_a, rs_a, mu_b, rs_b;
    rowstat(ga * 32 + r, mu_a, rs_a);
    rowstat(gb * 32 + r, mu_b, rs_b);
    const float cs_d = st_cs[(1 - wr) * 128 + wn * 32 + r];
    {
        auto wext = [&](float cs) {
            const float hi = bf_round(cs);
            u32x4 v = {h == 0 ? cvtpk(hi, cs - hi) : 0u, 0u, 0u, 0u};
            return __builtin_bit_cast(bf16x8, v);
        };
        auto xext = [&](float mu) {
            u32x4 v = {h == 0 ? cvtpk(-mu, -mu) : 0u, 0u, 0u, 0u};
            return __builtin_bit_cast(bf16x8, v);
        };
        const bf16x8 ec = wext(s_cs), ed = wext(cs_d), ea = xext(mu_a), eb = xext(mu_b);
        acc_ca = MFMA(ec, ea, acc_ca);
        acc_cb = MFMA(ec, eb, acc_cb);
        acc_da = MFMA(ed, ea, acc_da);
        acc_db = MFMA(ed, eb, acc_db);
        if (TYPE != 0) {
            const bf16x8 ek = wext(s_kv);
            if (TYPE == 1) acc_kv = MFMA(ek, ea, acc_kv); else acc_kv = MFMA(ea, ek, acc_kv);
        }
    }
    STAMP(0, 4);
    const int rga = rt * 4 + ga, rgb = rt * 4 + gb;
    if (TYPE == 0) {
        const float K1c = 2.0f * 0.7978845608028654f * 1.4426950408889634f, K3c = K1c * 0.044715f;
        char* dsta = ws + WS_ACT + ((size_t)rga * NKS + 32 + 2 * (4 * cg + wn)) * 1024 + lane * 16;
        char* dstb = ws + WS_ACT + ((size_t)rgb * NKS + 32 + 2 * (4 * cg + wn)) * 1024 + lane * 16;
        auto geglu = [&](const f32x16& G, const f32x16& U, float rs, char* dst) {
            const float c1 = K1c * rs, c3 = K3c * rs * rs * rs, ri2 = __builtin_amdgcn_rcpf(rs * rs);
            float hv[16];
#pragma unroll
            for (int i = 0; i < 16; ++i) {
                const float g = G[i];
                const float t = g * __builtin_fmaf(g * g, c3, c1);
                const float e = __builtin_amdgcn_exp2f(-t);
                const float rr = __builtin_amdgcn_rcpf(__builtin_fmaf(e, ri2, ri2));
                hv[i] = (g * U[i]) * rr;
            }
            u32x4 o0 = {cvtpk(hv[0], hv[1]), cvtpk(hv[2], hv[3]), cvtpk(hv[4], hv[5]), cvtpk(hv[6], hv[7])};
            u32x4 o1 = {cvtpk(hv[8], hv[9]), cvtpk(hv[10], hv[11]), cvtpk(hv[12], hv[13]), cvtpk(hv[14], hv[15])};
            *(u32x4*)dst = o0;
            *(u32x4*)(dst + 1024) = o1;
        };
        if (wr == 0) { geglu(acc_ca, acc_da, rs_a, dsta); geglu(acc_cb, acc_db, rs_b, dstb); }
        else         { geglu(acc_da, acc_ca, rs_a, dsta); geglu(acc_db, acc_cb, rs_b, dstb); }
    } else {
        const float qs = 0.25f * 1.4426950408889634f;
        const int hc = (TYPE - 1) * 16 + wr * 8 + wn * 2, hd = (TYPE - 1) * 16 + (1 - wr) * 8 + wn * 2;
        char* qa = ws + WS_QF + (size_t)rga * 32 * 1024 + lane * 16;
        char* qb = ws + WS_QF + (size_t)rgb * 32 * 1024 + lane * 16;
        *(u32x4*)(qa + hc * 1024) = pack8(acc_ca, 0, rs_a * qs);
        *(u32x4*)(qa + hc * 1024 + 1024) = pack8(acc_ca, 8, rs_a * qs);
        *(u32x4*)(qb + hc * 1024) = pack8(acc_cb, 0, rs_b * qs);
        *(u32x4*)(qb + hc * 1024 + 1024) = pack8(acc_cb, 8, rs_b * qs);
        *(u32x4*)(qa + hd * 1024) = pack8(acc_da, 0, rs_a * qs);
        *(u32x4*)(qa + hd * 1024 + 1024) = pack8(acc_da, 8, rs_a * qs);
        *(u32x4*)(qb + hd * 1024) = pack8(acc_db, 0, rs_b * qs);
        *(u32x4*)(qb + hd * 1024 + 1024) = pack8(acc_db, 8, rs_b * qs);
        if (kvw) {
            if (TYPE == 1) {
                *(u32x4*)(ws + WS_KF + (size_t)rga * 1024 + lane * 16) = pack8(acc_kv, 0, rs_a);
            } else {
                f32x16 v;
#pragma unroll
                for (int i = 0; i < 16; ++i) v[i] = acc_kv[i] * __shfl(rs_a, (i & 3) + 8 * (i >> 2) + 4 * h, 64);
                u32x4 o0 = pack8(v, 0, 1.0f), o1 = pack8(v, 8, 1.0f);
                if (r == 16) { o0 = (u32x4){0x3F803F80u, 0x3F803F80u, 0x3F803F80u, 0x3F803F80u}; o1 = o0; }
                if (r <= 16) {
                    char* vd = ws + WS_VF + (size_t)rga * 1088 + h * 272 + r * 16;
                    *(u32x4*)vd = o0;
                    *(u32x4*)(vd + 544) = o1;
                }
            }
        }
    }
}

__global__ __launch_bounds__(512, 2) void k1_proj(const u16* __restrict__ x, const u16* __restrict__ wq, const u16* __restrict__ wk,
                                                  const u16* __restrict__ wv, const u16* __restrict__ wo, const u16* __restrict__ wg,
                                                  const u16* __restrict__ wu, const u16* __restrict__ wd, char* __restrict__ ws) {
    __shared__ __attribute__((aligned(16))) char smem[163840];
    const int tid = threadIdx.x, lane = tid & 63;
    const int w = __builtin_amdgcn_readfirstlane(tid >> 6);
    const int xcd = blockIdx.x & 7, idx = blockIdx.x >> 3;
    const int rt = (xcd & 1) * 8 + (idx & 7), cg = (xcd >> 1) * 4 + (idx >> 3);
        const int r = lane & 31, h = lane >> 5;

    STAMP(0, 0);
    if (tid == 0) *(unsigned*)(ws + WS_FLAG + (size_t)blockIdx.x * 64) = 0u;
    u16 pw[8], pw2[8];
    const int f0 = blockIdx.x * 9 + w;
    {
        const int ng = f0 / NKS, ks = f0 % NKS;
        const u16* src = (ks < 32 ? wo + (size_t)ks * 16 * DM : wd + (size_t)(ks - 32) * 16 * DM) + ng * 32 + r + h * 4 * DM;
#pragma unroll
        for (int j = 0; j < 8; ++j) pw[j] = src[(8 * (j >> 2) + (j & 3)) * DM];
        const int f1 = blockIdx.x * 9 + 8, ng1 = f1 / NKS, ks1 = f1 % NKS;
        const u16* s1 = (ks1 < 32 ? wo + (size_t)ks1 * 16 * DM : wd + (size_t)(ks1 - 32) * 16 * DM) + ng1 * 32 + r + h * 4 * DM;
        if (w == 0) {
#pragma unroll
            for (int j = 0; j < 8; ++j) pw2[j] = s1[(8 * (j >> 2) + (j & 3)) * DM];
        } else {
#pragma unroll
            for (int j = 0; j < 8; ++j) pw2[j] = 0;
        }
    }
    asm volatile("" ::: "memory");
    if (cg < 14) k1_body<0>(x, wg + cg * 128, wu + cg * 128, wk, ws, smem, rt, cg, w, lane);
    else if (cg == 14) k1_body<1>(x, wq, wq + 128, wk, ws, smem, rt, cg, w, lane);
    else k1_body<2>(x, wq + 256, wq + 384, wv, ws, smem, rt, cg, w, lane);
    {
        u32x4 o;
#pragma unroll
        for (int i = 0; i < 4; ++i) o[i] = (unsigned)pw[2 * i] | ((unsigned)pw[2 * i + 1] << 16);
        *(u32x4*)(ws + WS_W2F + (size_t)f0 * 1024 + lane * 16) = o;
        if (w == 0) {
#pragma unroll
            for (int i = 0; i < 4; ++i) o[i] = (unsigned)pw2[2 * i] | ((unsigned)pw2[2 * i + 1] << 16);
            *(u32x4*)(ws + WS_W2F + ((size_t)blockIdx.x * 9 + 8) * 1024 + lane * 16) = o;
        }
    }
    STAMP(0, 5);
}

typedef __attribute__((address_space(1))) unsigned gu32;
#define SBAR() __builtin_amdgcn_sched_barrier(0)
#define EXP2(v) __builtin_amdgcn_exp2f(v)
#define BC8(v) __builtin_bit_cast(bf16x8, v)
DI void exp_pack(const f32x16& s, u32x4& p0, u32x4& p1) {
#pragma unroll
    for (int i = 0; i < 4; ++i) p0[i] = cvtpk(EXP2(s[2 * i]), EXP2(s[2 * i + 1]));
#pragma unroll
    for (int i = 0; i < 4; ++i) p1[i] = cvtpk(EXP2(s[8 + 2 * i]), EXP2(s[8 + 2 * i + 1]));
}
DI float max16(const f32x16& s) {
    float a = __builtin_fmaxf(s[0], s[1]);
#pragma unroll
    for (int i = 2; i < 16; i += 2) a = __builtin_fmaxf(__builtin_fmaxf(a, s[i]), s[i + 1]);
    return a;
}
DI f32x16 attn_slow(LAS const char* kp, LAS const char* vp, const bf16x8 q) {
    float m = -3.0e38f;
    f32x16 o = {};
#pragma unroll 1
    for (int t = 0; t < 64; ++t) {
        f32x16 z = {};
        f32x16 s = MFMA(lds_b128(kp + t * 1024), q, z);
        const float mn = __builtin_fmaxf(m, swap_max(max16(s)));
        const float al = EXP2(m - mn);
        m = mn;
#pragma unroll
        for (int i = 0; i < 16; ++i) { s[i] -= mn; o[i] *= al; }
        u32x4 p0, p1;
        exp_pack(s, p0, p1);
        o = MFMA(lds_b128(vp + t * 1088), BC8(p0), o);
        o = MFMA(lds_b128(vp + t * 1088 + 544), BC8(p1), o);
    }
    return o;
}

constexpr int K2_T0 = 10;
__global__ __launch_bounds__(512, 2) void k2_attn(const u16* __restrict__ x, char* __restrict__ ws, u16* __restrict__ out) {
    __shared__ __attribute__((aligned(16))) char smem[KV_BYTES + 1152];
    const int tid = threadIdx.x, lane = tid & 63;
    const int w = __builtin_amdgcn_readfirstlane(tid >> 6);
    const int xcd = blockIdx.x & 7, idx = blockIdx.x >> 3;
    const int it = xcd * 4 + (idx & 3), g = idx >> 2;
    const int r = lane & 31, h = lane >> 5;
    const unsigned lds0 = (unsigned)(uintptr_t)smem;
    const unsigned lo16 = lane * 16;
    LAS const char* L = (LAS const char*)smem;
    const __amdgpu_buffer_rsrc_t ra =
        __builtin_amdgcn_make_buffer_rsrc(ws + WS_ACT + (size_t)(2 * it) * NKS * 1024, 0, 2 * NKS * 1024, 0x00020000);
    gu32* flags = (gu32*)(ws + WS_FLAG) + it * 8 * 16;
    STAMP(1, 0);
    if (w < 4) {
        const int head = 4 * g + w;
        const bf16x8 qa = *(const bf16x8*)(ws + WS_QF + ((size_t)(2 * it) * 32 + head) * 1024 + lo16);
        const bf16x8 qb = *(const bf16x8*)(ws + WS_QF + ((size_t)(2 * it + 1) * 32 + head) * 1024 + lo16);
        LAS const char* kp = L + lo16;
        LAS const char* vp = L + 65536 + h * 272 + (r < 16 ? r : 16) * 16;
        asm volatile("s_barrier" ::: "memory");
        STAMP(1, 1);
        f32x16 nma, nmb;
        bf16x8 kf = lds_b128(kp);
        bf16x8 v0 = lds_b128(vp), v1 = lds_b128(vp + 544);
        {
            f32x16 z = {};
            f32x16 s0 = MFMA(kf, qa, z), s1 = MFMA(kf, qb, z);
            const float ma = swap_max(max16(s0)), mb = swap_max(max16(s1));
#pragma unroll
            for (int i = 0; i < 16; ++i) { nma[i] = -ma; nmb[i] = -mb; }
        }
        f32x16 oa = {}, ob = {};
        f32x16 sa = MFMA(kf, qa, nma), sb;
        float e0, e1;
        u32x4 pp;
        bf16x8 vq = v1;
        pp = (u32x4){0u, 0u, 0u, 0u};
        e0 = EXP2(sa[0]); e1 = EXP2(sa[1]);
#define ATT_HALF(SX, OX, OP, SY, NEXT, V0, V1, LOADS)                                                   \
    do {                                                                                                \
        OP = MFMA(vq, BC8(pp), OP); SBAR();                                                             \
        float e2 = EXP2(SX[2]), e3 = EXP2(SX[3]); LOADS; float e4 = EXP2(SX[4]), e5 = EXP2(SX[5]); SBAR(); \
        SY = NEXT; SBAR();                                                                              \
        float e6 = EXP2(SX[6]), e7 = EXP2(SX[7]);                                                       \
        u32x4 p0; p0[0] = cvtpk(e0, e1); p0[1] = cvtpk(e2, e3); p0[2] = cvtpk(e4, e5); p0[3] = cvtpk(e6, e7); SBAR(); \
        e0 = EXP2(SX[8]); e1 = EXP2(SX[9]); SBAR();                                                     \
        OX = MFMA(V0, BC8(p0), OX); SBAR();                                                             \
        e2 = EXP2(SX[10]); e3 = EXP2(SX[11]); e4 = EXP2(SX[12]); e5 = EXP2(SX[13]); e6 = EXP2(SX[14]); e7 = EXP2(SX[15]); \
        pp[0] = cvtpk(e0, e1); pp[1] = cvtpk(e2, e3); pp[2] = cvtpk(e4, e5); pp[3] = cvtpk(e6, e7); vq = V1; SBAR(); \
        e0 = EXP2(SY[0]); e1 = EXP2(SY[1]); SBAR();                                                     \
    } while (0)
#define ATT_TILE(t)                                                                                     \
    do {                                                                                                \
        bf16x8 kn, n0, n1;                                                                              \
        ATT_HALF(sa, oa, ob, sb, MFMA(kf, qb, nmb), v0, v1, kn = lds_b128(kp + ((t) + 1) * 1024));      \
        ATT_HALF(sb, ob, oa, sa, MFMA(kn, qa, nma), v0, v1,                                             \
                 (n0 = lds_b128(vp + ((t) + 1) * 1088), n1 = lds_b128(vp + ((t) + 1) * 1088 + 544)));   \
        kf = kn; v0 = n0; v1 = n1;                                                                      \
    } while (0)
#pragma unroll 1
        for (int t = 0; t < K2_T0; t += 2) { ATT_TILE(t); ATT_TILE(t + 1); }
        asm volatile("s_waitcnt lgkmcnt(0)\n\ts_barrier" ::: "memory");
        STAMP(1, 2);
#pragma unroll 1
        for (int t = K2_T0; t < 64; t += 2) { ATT_TILE(t); ATT_TILE(t + 1); }
        ob = MFMA(vq, BC8(pp), ob);
        STAMP(1, 3);
        {
            const float la = oa[8], lb = ob[8];
            const bool bad = !(la > 9.094947e-13f && la < 1.0995116e12f && lb > 9.094947e-13f && lb < 1.0995116e12f);
            if (__builtin_expect(__any(bad), 0)) { oa = attn_slow(kp, vp, qa); ob = attn_slow(kp, vp, qb); }
        }
        __builtin_amdgcn_raw_buffer_store_b128(pack8(oa, 0, 1.0f / oa[8]), ra, lo16, head * 1024, 16);
        __builtin_amdgcn_raw_buffer_store_b128(pack8(ob, 0, 1.0f / ob[8]), ra, lo16, (NKS + head) * 1024, 16);
        asm volatile("s_waitcnt vmcnt(0) lgkmcnt(0)\n\ts_barrier" ::: "memory");
        STAMP(1, 4);
    } else {
        const int gw = w - 4;
        const char* src = ws + WS_KF;
#pragma unroll
        for (int i = 0; i < 3; ++i) glds16s(src + (gw + 4 * i) * 1024, lo16, lds0 + (gw + 4 * i) * 1024);
#pragma unroll
        for (int i = 0; i < 4; ++i) glds16s(src + (64 + gw + 4 * i) * 1024, lo16, lds0 + (64 + gw + 4 * i) * 1024);
#pragma unroll
        for (int i = 3; i < 16; ++i) glds16s(src + (gw + 4 * i) * 1024, lo16, lds0 + (gw + 4 * i) * 1024);
#pragma unroll
        for (int i = 4; i < 17; ++i) glds16s(src + (64 + gw + 4 * i) * 1024, lo16, lds0 + (64 + gw + 4 * i) * 1024);
        WAIT_BAR(26);
        STAMP(1, 1);
        WAIT_BAR(0);
        STAMP(1, 2);
        const __amdgpu_buffer_rsrc_t rw =
            __builtin_amdgcn_make_buffer_rsrc(ws + WS_W2F + (size_t)(2 * g) * NKS * 1024, 0, 2 * NKS * 1024, 0x00020000);
        const int so = (32 + 28 * gw) * 1024;
        constexpr int PD = 6;
        f32x16 acc00 = {}, acc01 = {}, acc10 = {}, acc11 = {};
        u32x4 fw0[8], fw1[8], fa0[8], fa1[8];
#define G_LOAD(i, k)                                                                                   \
    do {                                                                                               \
        fw0[i] = __builtin_amdgcn_raw_buffer_load_b128(rw, lo16, so + (k) * 1024, 0);                  \
        fa0[i] = __builtin_amdgcn_raw_buffer_load_b128(ra, lo16, so + (k) * 1024, 0);                  \
        fa1[i] = __builtin_amdgcn_raw_buffer_load_b128(ra, lo16, so + (NKS + (k)) * 1024, 0);          \
        fw1[i] = __builtin_amdgcn_raw_buffer_load_b128(rw, lo16, so + (NKS + (k)) * 1024, 0);          \
        SBAR();                                                                                        \
    } while (0)
#define G_MMA(i)                                                                                       \
    do {                                                                                               \
        acc00 = MFMA(BC8(fw0[i]), BC8(fa0[i]), acc00); acc01 = MFMA(BC8(fw0[i]), BC8(fa1[i]), acc01);  \
        acc10 = MFMA(BC8(fw1[i]), BC8(fa0[i]), acc10); acc11 = MFMA(BC8(fw1[i]), BC8(fa1[i]), acc11);  \
        SBAR();                                                                                        \
    } while (0)
#pragma unroll
        for (int i = 0; i < PD; ++i) G_LOAD(i, i);
#pragma unroll
        for (int k = 0; k < 28; ++k) {
            G_MMA(k % PD);
            if (k + PD < 28) G_LOAD(k % PD, k + PD);
        }
#pragma unroll
        for (int i = 0; i < 8; ++i) {
            fw0[i] = __builtin_amdgcn_raw_buffer_load_b128(rw, lo16, (8 * gw + i) * 1024, 0);
            fw1[i] = __builtin_amdgcn_raw_buffer_load_b128(rw, lo16, (NKS + 8 * gw + i) * 1024, 0);
        }
        STAMP(1, 3);
        asm volatile("s_barrier" ::: "memory");
        STAMP(1, 4);
        if (tid == 256) __hip_atomic_store(flags + g * 16, 1u, __ATOMIC_RELAXED, __HIP_MEMORY_SCOPE_AGENT);
        {
            gu32* fl = flags + (2 * gw + (lane & 1)) * 16;
            for (unsigned spins = 0; spins < (1u << 24); ++spins) {
                const unsigned v = __hip_atomic_load(fl, __ATOMIC_RELAXED, __HIP_MEMORY_SCOPE_AGENT);
                if (__all(v == 1u)) break;
                __builtin_amdgcn_s_sleep(1);
            }
            __builtin_amdgcn_fence(__ATOMIC_ACQUIRE, "wavefront");
            asm volatile("" ::: "memory");
        }
        STAMP(1, 5);
#pragma unroll
        for (int i = 0; i < 8; ++i) {
            fa0[i] = __builtin_amdgcn_raw_buffer_load_b128(ra, lo16, (8 * gw + i) * 1024, 16);
            fa1[i] = __builtin_amdgcn_raw_buffer_load_b128(ra, lo16, (NKS + 8 * gw + i) * 1024, 16);
        }
        SBAR();
#pragma unroll
        for (int i = 0; i < 8; ++i) G_MMA(i);
#undef G_LOAD
#undef G_MMA
        char* dst = smem + gw * 16640 + lo16;
#pragma unroll
        for (int q = 0; q < 4; ++q) {
            *(f32x4*)(dst + (0 + q) * 1040) = (f32x4){acc00[4 * q], acc00[4 * q + 1], acc00[4 * q + 2], acc00[4 * q + 3]};
            *(f32x4*)(dst + (4 + q) * 1040) = (f32x4){acc01[4 * q], acc01[4 * q + 1], acc01[4 * q + 2], acc01[4 * q + 3]};
            *(f32x4*)(dst + (8 + q) * 1040) = (f32x4){acc10[4 * q], acc10[4 * q + 1], acc10[4 * q + 2], acc10[4 * q + 3]};
            *(f32x4*)(dst + (12 + q) * 1040) = (f32x4){acc11[4 * q], acc11[4 * q + 1], acc11[4 * q + 2], acc11[4 * q + 3]};
        }
        STAMP(1, 6);
    }
    const int row = tid >> 3, c8 = tid & 7;
    const size_t e = (size_t)(it * 64 + row) * DM + g * 64 + c8 * 8;
    const u32x4 xv = *(const u32x4*)(x + e);
    __syncthreads();
    {
        const int a = c8 >> 2, q = c8 & 3, b = row >> 5, rr = row & 31;
        const char* s0 = smem + ((a * 2 + b) * 4 + q) * 1040 + rr * 16;
        f32x4 lo = {__uint_as_float(xv[0] << 16), __uint_as_float(xv[0] & 0xffff0000u), __uint_as_float(xv[1] << 16), __uint_as_float(xv[1] & 0xffff0000u)};
        f32x4 hi = {__uint_as_float(xv[2] << 16), __uint_as_float(xv[2] & 0xffff0000u), __uint_as_float(xv[3] << 16), __uint_as_float(xv[3] & 0xffff0000u)};
#pragma unroll
        for (int gw = 0; gw < 4; ++gw) {
            lo += *(const f32x4*)(s0 + gw * 16640);
            hi += *(const f32x4*)(s0 + gw * 16640 + 512);
        }
        u32x4 o = {cvtpk(lo[0], lo[1]), cvtpk(lo[2], lo[3]), cvtpk(hi[0], hi[1]), cvtpk(hi[2], hi[3])};
        *(u32x4*)(out + e) = o;
    }
    STAMP(1, 7);
}

extern "C" void kernel_launch(void* const* d_in, const int* in_sizes, int n_in, void* d_out, int out_size, void* d_ws,
                              size_t ws_size, hipStream_t stream) {
    const u16* x = (const u16*)d_in[0];
    const u16* wq = (const u16*)d_in[1];
    const u16* wk = (const u16*)d_in[2];
    const u16* wv = (const u16*)d_in[3];
    const u16* wo = (const u16*)d_in[4];
    const u16* wg = (const u16*)d_in[6];
    const u16* wu = (const u16*)d_in[7];
    const u16* wd = (const u16*)d_in[8];
    char* ws = (char*)d_ws;
    k1_proj<<<256, 512, 0, stream>>>(x, wq, wk, wv, wo, wg, wu, wd, ws);
    k2_attn<<<256, 512, 0, stream>>>(x, ws, (u16*)d_out);
}
```

```cpp
#include <hip/hip_runtime.h>
#include <hip/hip_bf16.h>
#include <stdint.h>

typedef __bf16 bf16x8 __attribute__((ext_vector_type(8)));
typedef __bf16 bf16x2 __attribute__((ext_vector_type(2)));
typedef short s16x4 __attribute__((ext_vector_type(4)));
typedef float f32x16 __attribute__((ext_vector_type(16)));
typedef float f32x4 __attribute__((ext_vector_type(4)));
typedef unsigned u32x4 __attribute__((ext_vector_type(4)));
typedef unsigned u32x2 __attribute__((ext_vector_type(2)));
typedef unsigned short u16;
typedef unsigned long long u64;
typedef __attribute__((address_space(1))) u64 gu64;
#define LAS __attribute__((address_space(3)))
#define DI __device__ __forceinline__
#define MFMA(a, b, c) __builtin_amdgcn_mfma_f32_32x32x16_bf16((a), (b), (c), 0, 0, 0)
#define MFMA16(a, b, c) __builtin_amdgcn_mfma_f32_16x16x32_bf16((a), (b), (c), 0, 0, 0)
#define SBAR() __builtin_amdgcn_sched_barrier(0)
#define EXP2(v) __builtin_amdgcn_exp2f(v)
#define BC8(v) __builtin_bit_cast(bf16x8, v)
#define SC1 16

constexpr int DM = 512, DF = 1792;
constexpr int NKS = 144;
constexpr size_t WS_KF = 0;
constexpr size_t WS_VF = 65536;
constexpr size_t WS_STAT = 131072;
constexpr size_t WS_SYNC = 131072 + 16384;
constexpr size_t SY_EPOCH = 0, SY_SEEN = 4096, SY_W = 8192, SY_KV = 16384, SY_H = 32768, SY_O = 65536;
constexpr int FL = 128;
constexpr size_t WS_ACT = 262144;
constexpr size_t WS_W2F = WS_ACT + (size_t)64 * NKS * 1024;
constexpr size_t WS_END = WS_W2F + (size_t)16 * NKS * 1024;
constexpr unsigned SPIN_CAP = 1u << 18;
#define G1PRIO 0
#define ROTG 0
#define ROTA 0

#define STAMP(kern, i)

DI unsigned cvtpk(float lo, float hi) {
    bf16x2 r = {(__bf16)lo, (__bf16)hi};
    return __builtin_bit_cast(unsigned, r);
}
DI float bf_round(float v) { return (float)(__bf16)v; }
DI float swap_add(float v) {
    auto rr = __builtin_amdgcn_permlane32_swap(__float_as_uint(v), __float_as_uint(v), false, false);
    return __uint_as_float(rr[0]) + __uint_as_float(rr[1]);
}
DI float swap_max(float v) {
    auto rr = __builtin_amdgcn_permlane32_swap(__float_as_uint(v), __float_as_uint(v), false, false);
    return __builtin_fmaxf(__uint_as_float(rr[0]), __uint_as_float(rr[1]));
}
DI void glds16s(const void* sbase, unsigned voff, unsigned lds_base) {
    asm volatile("s_mov_b32 m0, %2\n\ts_nop 0\n\tglobal_load_lds_dwordx4 %0, %1" ::"v"(voff), "s"(sbase), "s"(lds_base) : "memory");
    SBAR();
}
template <int N> DI void wait_bar() { asm volatile("s_waitcnt vmcnt(%0) lgkmcnt(0)\n\ts_barrier" ::"n"(N) : "memory"); }
DI bf16x8 lds_b128(LAS const char* p) { return *(LAS const bf16x8*)p; }
template <int STEP> DI bf16x8 lds_tr2(LAS const char* p) {
    s16x4 lo = __builtin_amdgcn_ds_read_tr16_b64_v4i16((LAS s16x4*)p);
    s16x4 hi = __builtin_amdgcn_ds_read_tr16_b64_v4i16((LAS s16x4*)(p + STEP));
    return __builtin_bit_cast(bf16x8, __builtin_shufflevector(lo, hi, 0, 1, 2, 3, 4, 5, 6, 7));
}
template <int N> struct IC { static constexpr int value = N; };
DI u32x4 pack8(const f32x16& v, int o, float s) {
    u32x4 r;
#pragma unroll
    for (int i = 0; i < 4; ++i) r[i] = cvtpk(v[o + 2 * i] * s, v[o + 2 * i + 1] * s);
    return r;
}
DI void ln_stat(float sum, float sq, float& mu, float& rs) {
    const float mean = sum * (1.0f / DM);
    const float var = __builtin_fmaxf(sq * (1.0f / DM) - mean * mean, 0.f);
    const float sd = bf_round(__builtin_amdgcn_sqrtf(bf_round(bf_round(var) + 1e-5f)));
    mu = bf_round(mean); rs = __builtin_amdgcn_rcpf(sd);
}
DI bf16x8 wext(float cs, int h) {
    const float hi = bf_round(cs);
    u32x4 v = {h == 0 ? cvtpk(hi, cs - hi) : 0u, 0u, 0u, 0u};
    return BC8(v);
}
DI bf16x8 xext(float mu, int h) {
    u32x4 v = {h == 0 ? cvtpk(-mu, -mu) : 0u, 0u, 0u, 0u};
    return BC8(v);
}
DI bf16x8 make_sel(int lane) {
    const unsigned o1 = ((lane & 3) == 0 && ((lane >> 2) & 1) == ((lane >> 4) & 1)) ? 0x3F803F80u : 0u;
    return BC8(((u32x4){o1, o1, o1, o1}));
}
DI u64 load_epoch(const char* ws) {
    u64 e;
    asm volatile("s_load_dwordx2 %0, %1, 0x0\n\ts_waitcnt lgkmcnt(0)" : "=s"(e) : "s"(ws + WS_SYNC + SY_EPOCH) : "memory");
    return e;
}
DI void set_flag(char* ws, size_t off, u64 tag) {
    __hip_atomic_store((gu64*)(ws + WS_SYNC + off), tag, __ATOMIC_RELAXED, __HIP_MEMORY_SCOPE_AGENT);
}
DI void wait_flags(char* ws, unsigned off, u64 tag) {
    gu64* p = (gu64*)(ws + WS_SYNC + off);
    for (unsigned spins = 0; spins < SPIN_CAP; ++spins) {
        const u64 v = __hip_atomic_load(p, __ATOMIC_RELAXED, __HIP_MEMORY_SCOPE_AGENT);
        if (__all(v == tag)) break;
        __builtin_amdgcn_s_sleep(2);
    }
    __builtin_amdgcn_fence(__ATOMIC_ACQUIRE, "wavefront");
    asm volatile("" ::: "memory");
}
DI float max16(const f32x16& s) {
    float a = __builtin_fmaxf(s[0], s[1]);
#pragma unroll
    for (int i = 2; i < 16; i += 2) a = __builtin_fmaxf(__builtin_fmaxf(a, s[i]), s[i + 1]);
    return a;
}

DI f32x16 attn_slow(const __amdgpu_buffer_rsrc_t rkv, const bf16x8 q, const int lane) {
    const int r = lane & 31, h = lane >> 5;
    const unsigned vo = (unsigned)WS_VF + h * 256 + (r & 15) * 16;
    float m = -3.0e38f;
    f32x16 o = {};
#pragma unroll 1
    for (int t = 0; t < 64; ++t) {
        const u32x4 kf = __builtin_amdgcn_raw_buffer_load_b128(rkv, lane * 16, t * 1024, SC1);
        u32x4 v0 = __builtin_amdgcn_raw_buffer_load_b128(rkv, vo, t * 1024, SC1);
        u32x4 v1 = __builtin_amdgcn_raw_buffer_load_b128(rkv, vo + 512, t * 1024, SC1);
        if (r >= 16) { v0 = (u32x4){0x3F803F80u, 0x3F803F80u, 0x3F803F80u, 0x3F803F80u}; v1 = v0; }
        f32x16 z = {};
        f32x16 s = MFMA(BC8(kf), q, z);
        const float mn = __builtin_fmaxf(m, swap_max(max16(s)));
        const float al = EXP2(m - mn);
        m = mn;
#pragma unroll
        for (int i = 0; i < 16; ++i) { s[i] -= mn; o[i] *= al; }
        u32x4 p0, p1;
#pragma unroll
        for (int i = 0; i < 4; ++i) {
            p0[i] = cvtpk(EXP2(s[2 * i]), EXP2(s[2 * i + 1]));
            p1[i] = cvtpk(EXP2(s[8 + 2 * i]), EXP2(s[8 + 2 * i + 1]));
        }
        o = MFMA(BC8(v0), BC8(p0), o);
        o = MFMA(BC8(v1), BC8(p1), o);
    }
    return o;
}

constexpr int A_STAGE = 16384, A_KVW = 65536;
constexpr int A_SLOT = 8 * 1024 + 8 * 1088;
DI void role_a(const u16* __restrict__ x, const u16* __restrict__ wq, const u16* __restrict__ wk, const u16* __restrict__ wv,
               char* __restrict__ ws, char* smem, const int bid, const int w, const int lane) {
    const int xcd = bid & 7, idx = bid >> 3;
    const int it = xcd * 4 + (idx & 3), g = idx >> 2;
    const int p = w >> 1, rgl = w & 1;
    const int r = lane & 31, h = lane >> 5;
    const unsigned lds0 = (unsigned)(uintptr_t)smem;
    const unsigned lo16 = lane * 16;
    LAS const char* L = (LAS const char*)smem;
    const bool hasjob = g < 4;
    const bool jobw = hasjob && (rgl == (g >> 1));
    const bool isv = g & 1;
    __builtin_amdgcn_s_setprio(3);
    STAMP(0, 0);

    const int prow = lane >> 3, pslot = lane & 7;
    const unsigned xo0 = (unsigned)((16 * w + prow) * DM * 2 + ((pslot ^ (((16 * w + prow) >> 1) & 7)) * 16));
    const unsigned xo1 = (unsigned)((16 * w + 8 + prow) * DM * 2 + ((pslot ^ (((16 * w + 8 + prow) >> 1) & 7)) * 16));
    const unsigned wo0 = (unsigned)((16 * w + prow) * DM * 2 + ((pslot ^ (((prow >> 1) & 1) << 2)) * 16));
    const unsigned wo1 = wo0 + 8 * DM * 2;
    const char* xb = (const char*)(x + (size_t)it * 64 * DM);
    const char* wb = (const char*)(wq + g * 64);
    const int rot = ROTA ? (g + 2 * (idx & 3)) & 7 : 0;
#define A_DMA(s, i)                                                                                       \
    do {                                                                                                  \
        if ((s) < 8) {                                                                                    \
            const unsigned sb_ = lds0 + ((s) & 3) * A_STAGE + w * 2048;                                   \
            const int sr_ = ((s) + rot) & 7;                                                              \
            if ((i) == 0) glds16s(xb + sr_ * 128, xo0, sb_);                                              \
            if ((i) == 1) glds16s(xb + sr_ * 128, xo1, sb_ + 1024);                                       \
            if ((i) == 2) glds16s(wb + sr_ * (64 * DM * 2), wo0, sb_ + 8192);                             \
            if ((i) == 3) glds16s(wb + sr_ * (64 * DM * 2), wo1, sb_ + 8192 + 1024);                      \
        }                                                                                                 \
    } while (0)
#pragma unroll
    for (int i = 0; i < 4; ++i) A_DMA(0, i);
    if (hasjob) {
        const char* kvw = (const char*)(isv ? wv : wk) + w * 4096;
#pragma unroll
        for (int i = 0; i < 4; ++i) glds16s(kvw + i * 1024, lo16, lds0 + A_KVW + w * 4096 + i * 1024);
    }
#pragma unroll
    for (int s = 1; s < 4; ++s)
#pragma unroll
        for (int i = 0; i < 4; ++i) A_DMA(s, i);
    const u64 tag = load_epoch(ws) + 1;

    const int fx = ((r >> 1) & 7) ^ (p << 1);
    const int blk = (lane >> 4) & 1, q4 = (lane & 15) >> 2, p4 = lane & 3;
    const int wlane = 8192 + h * 1024 + q4 * 128 + (((p * 4 + blk * 2 + (p4 >> 1)) ^ (((q4 >> 1) & 1) << 2)) * 16) + (p4 & 1) * 8;
    const int kvlane = A_KVW + (8 * h + q4) * 32 + p4 * 8;
    const bf16x8 sel = make_sel(lane);
    f32x16 accq = {}, acck = {}, accx = {};
    f32x4 acc_cs = {}, acc_ck = {}, acc_rs = {};
    bf16x8 xa[2], wc[2], kf_;
#define A_READ(set, s, j)                                                             \
    do {                                                                              \
        LAS const char* sb_ = L + ((s) & 3) * A_STAGE;                                \
        const int ks_ = (j) + (((j) & 1) ? -p : p);                                   \
        wc[set] = lds_tr2<512>(sb_ + wlane + ks_ * 2048);                             \
        xa[set] = lds_b128(sb_ + (rgl * 32 + r) * 128 + (((2 * (j) + h) ^ fx) * 16)); \
        if (JOB != 0 && ((j) & 1) != 0) kf_ = lds_tr2<128>(L + kvlane + (((s) + rot) & 7) * 2048 + ks_ * 512); \
        SBAR();                                                                       \
    } while (0)
#define A_MMA(set, j, ds, d0)                                                         \
    do {                                                                              \
        accq = MFMA(wc[set], xa[set], accq);                                          \
        acc_cs = MFMA16(sel, wc[set], acc_cs);                                        \
        if (((j) & 1) == 0) {                                                         \
            accx = MFMA(xa[set], xa[set], accx);                                      \
            acc_rs = MFMA16(sel, xa[set], acc_rs);                                    \
        } else if (JOB) {                                                             \
            if (JOB == 2) acck = MFMA(xa[set], kf_, acck); else acck = MFMA(kf_, xa[set], acck); \
            acc_ck = MFMA16(sel, kf_, acc_ck);                                        \
        }                                                                             \
        SBAR();                                                                       \
        A_DMA(ds, d0);                                                                \
    } while (0)
    STAMP(0, 1);
    wait_bar<12>();
    STAMP(0, 2);
    if (lane == 0 && w == 1) set_flag(ws, SY_SEEN + (size_t)bid * 8, tag);
    auto a1_loop = [&](auto jobc) {
        constexpr int JOB = decltype(jobc)::value;
        A_READ(0, 0, 0);
#pragma unroll
        for (int s = 0; s < 8; ++s) {
            A_READ(1, s, 1); A_MMA(0, 0, (s >= 1 ? s + 3 : 8), 1);
            A_READ(0, s, 2); A_MMA(1, 1, (s >= 1 ? s + 3 : 8), 2);
            A_READ(1, s, 3); A_MMA(0, 2, (s >= 1 ? s + 3 : 8), 3);
            if (s < 7) {
                if (s <= 4) wait_bar<8>(); else if (s == 5) wait_bar<4>(); else wait_bar<0>();
                A_READ(0, s + 1, 0);
            }
            A_MMA(1, 3, s + 4, 0);
        }
    };
    if (!jobw) a1_loop(IC<0>{}); else if (!isv) a1_loop(IC<1>{}); else a1_loop(IC<2>{});
#undef A_READ
#undef A_MMA
#undef A_DMA
    STAMP(0, 3);
    bf16x8 qa, qb;
    {
        typedef float f32x8 __attribute__((ext_vector_type(8)));
        typedef float f32x2 __attribute__((ext_vector_type(2)));
        const f32x8 t8 = (r & 16) ? __builtin_shufflevector(accx, accx, 8, 9, 10, 11, 12, 13, 14, 15)
                                  : __builtin_shufflevector(accx, accx, 0, 1, 2, 3, 4, 5, 6, 7);
        const f32x4 t4 = (r & 8) ? __builtin_shufflevector(t8, t8, 4, 5, 6, 7) : __builtin_shufflevector(t8, t8, 0, 1, 2, 3);
        const f32x2 t2 = (r & 2) ? __builtin_shufflevector(t4, t4, 2, 3) : __builtin_shufflevector(t4, t4, 0, 1);
        const float t20 = t2[0], t21 = t2[1];
        const float dg = (r & 1) ? t21 : t20;
        float* st = (float*)smem;
        if (h == ((r >> 2) & 1)) { st[p * 64 + rgl * 32 + r] = acc_rs[0]; st[128 + p * 64 + rgl * 32 + r] = dg; }
        if (jobw && p == 1) {
            if (lane < 16) st[256 + lane] = acc_ck[0];
#pragma unroll
            for (int q = 0; q < 4; ++q)
                *(f32x4*)(smem + 2048 + q * 1024 + lo16) = (f32x4){acck[4 * q], acck[4 * q + 1], acck[4 * q + 2], acck[4 * q + 3]};
        }
        __syncthreads();
        float mu, rs;
        ln_stat(st[rgl * 32 + r] + st[64 + rgl * 32 + r], st[128 + rgl * 32 + r] + st[192 + rgl * 32 + r], mu, rs);
        const bf16x8 ex = xext(mu, h);
        if (jobw && p == 0) {
            const __amdgpu_buffer_rsrc_t rkv_st = __builtin_amdgcn_make_buffer_rsrc(ws, 0, 131072 + 16384, 0x00020000);
            const bf16x8 ek = wext(acc_ck[0] + st[256 + (lane & 15)], h);
            const int rg = 2 * it + rgl;
#pragma unroll
            for (int q = 0; q < 4; ++q) {
                const f32x4 o = *(const f32x4*)(smem + 2048 + q * 1024 + lo16);
                acck[4 * q] += o[0]; acck[4 * q + 1] += o[1]; acck[4 * q + 2] += o[2]; acck[4 * q + 3] += o[3];
            }
            if (isv) {
                if (h == 0) st[272 + r] = rs;
                acck = MFMA(ex, ek, acck);
                f32x16 v;
#pragma unroll
                for (int q = 0; q < 4; ++q) {
                    const f32x4 rr = *(const f32x4*)(st + 272 + 8 * q + 4 * h);
#pragma unroll
                    for (int i = 0; i < 4; ++i) v[4 * q + i] = acck[4 * q + i] * rr[i];
                }
                const unsigned vo = r < 16 ? (unsigned)WS_VF + h * 256 + r * 16 : 0x40000000u;
                __builtin_amdgcn_raw_buffer_store_b128(pack8(v, 0, 1.0f), rkv_st, vo + rg * 1024, 0, SC1);
                __builtin_amdgcn_raw_buffer_store_b128(pack8(v, 8, 1.0f), rkv_st, vo + rg * 1024 + 512, 0, SC1);
            } else {
                acck = MFMA(ek, ex, acck);
                u32x2 ms = {__float_as_uint(mu), __float_as_uint(rs)};
                __builtin_amdgcn_raw_buffer_store_b64(ms, rkv_st, h == 0 ? (unsigned)WS_STAT + rg * 256 + r * 8 : 0x40000000u, 0, SC1);
                __builtin_amdgcn_raw_buffer_store_b128(pack8(acck, 0, rs), rkv_st, lo16 + rg * 1024, 0, SC1);
            }
            STAMP(0, 9);
            asm volatile("s_waitcnt vmcnt(0)" ::: "memory");
            STAMP(0, 10);
            if (lane == 0) set_flag(ws, SY_KV + (size_t)(rg * 2 + (isv ? 1 : 0)) * FL, tag);
        }
        accq = MFMA(wext(acc_cs[0], h), ex, accq);
        const float qs = rs * (0.25f * 1.4426950408889634f);
        qa = BC8(pack8(accq, 0, qs));
        qb = BC8(pack8(accq, 8, qs));
    }
    STAMP(0, 4);

    const __amdgpu_buffer_rsrc_t rkv = __builtin_amdgcn_make_buffer_rsrc(ws, 0, 131072, 0x00020000);
    if (w == 3) {
        gu64* f0 = (gu64*)(ws + WS_SYNC + SY_KV + lane * FL);
        gu64* f1 = (gu64*)(ws + WS_SYNC + SY_KV + (64 + lane) * FL);
        for (unsigned spins = 0; spins < SPIN_CAP; ++spins) {
            const u64 a0 = __hip_atomic_load(f0, __ATOMIC_RELAXED, __HIP_MEMORY_SCOPE_AGENT);
            const u64 a1 = __hip_atomic_load(f1, __ATOMIC_RELAXED, __HIP_MEMORY_SCOPE_AGENT);
            if (__all(a0 == tag && a1 == tag)) break;
            __builtin_amdgcn_s_sleep(1);
        }
        __builtin_amdgcn_fence(__ATOMIC_ACQUIRE, "wavefront");
        asm volatile("" ::: "memory");
    }
    asm volatile("s_waitcnt lgkmcnt(0)\n\ts_barrier" ::: "memory");
    STAMP(0, 5);
    u32x4 sk0, sk1, sv0, sv1;
#define KV_LOAD(c)                                                                                     \
    do {                                                                                               \
        const int so_ = ((c) * 8 + 2 * w) * 1024;                                                      \
        sk0 = __builtin_amdgcn_raw_buffer_load_b128(rkv, lo16, so_, SC1);                              \
        sk1 = __builtin_amdgcn_raw_buffer_load_b128(rkv, lo16 + 1024, so_, SC1);                       \
        sv0 = __builtin_amdgcn_raw_buffer_load_b128(rkv, lo16 + (unsigned)WS_VF, so_, SC1);            \
        sv1 = __builtin_amdgcn_raw_buffer_load_b128(rkv, lo16 + (unsigned)WS_VF + 1024, so_, SC1);     \
    } while (0)
    const int kwr = w * 2048 + lo16, vwr = 8192 + w * 2176 + (lane >> 4) * 272 + (lane & 15) * 16;
#define KV_WRITE(slotbase)                                                                             \
    do {                                                                                               \
        *(LAS u32x4*)(slotbase + kwr) = sk0; *(LAS u32x4*)(slotbase + kwr + 1024) = sk1;               \
        *(LAS u32x4*)(slotbase + vwr) = sv0; *(LAS u32x4*)(slotbase + vwr + 1088) = sv1;               \
    } while (0)
    LAS char* LW = (LAS char*)smem;
    {
        u32x4 tk0, tk1, tv0, tv1;
        KV_LOAD(0);
        tk0 = sk0; tk1 = sk1; tv0 = sv0; tv1 = sv1;
        KV_LOAD(1);
        if (threadIdx.x < 96) {
            const int s3 = threadIdx.x >> 5, e = threadIdx.x & 31;
            *(LAS u32x4*)(LW + s3 * A_SLOT + 8192 + e * 272 + 256) = (u32x4){0x3F803F80u, 0x3F803F80u, 0x3F803F80u, 0x3F803F80u};
        }
        *(LAS u32x4*)(LW + kwr) = tk0; *(LAS u32x4*)(LW + kwr + 1024) = tk1;
        *(LAS u32x4*)(LW + vwr) = tv0; *(LAS u32x4*)(LW + vwr + 1088) = tv1;
        KV_WRITE((LW + A_SLOT));
        KV_LOAD(2);
    }
    asm volatile("s_waitcnt lgkmcnt(0)\n\ts_barrier" ::: "memory");
    STAMP(0, 6);
    const int kln = lo16, vln = 8192 + h * 272 + (r < 16 ? r : 16) * 16;
    f32x16 nma, nmb;
    bf16x8 kf = lds_b128(L + kln);
    bf16x8 v0 = lds_b128(L + vln), v1 = lds_b128(L + vln + 544);
    {
        f32x16 z = {};
        f32x16 s0 = MFMA(kf, qa, z), s1 = MFMA(kf, qb, z);
        const float ma = swap_max(max16(s0)), mb = swap_max(max16(s1));
#pragma unroll
        for (int i = 0; i < 16; ++i) { nma[i] = -ma; nmb[i] = -mb; }
    }
    f32x16 oa = {}, ob = {};
    f32x16 sa = MFMA(kf, qa, nma), sb;
    float e0, e1;
    u32x4 pp = {0u, 0u, 0u, 0u};
    bf16x8 vq = v1;
    e0 = EXP2(sa[0]); e1 = EXP2(sa[1]);
#define ATT_HALF(SX, OX, OP, SY, NEXT, V0, V1, LOADS)                                                   \
    do {                                                                                                \
        OP = MFMA(vq, BC8(pp), OP); SBAR();                                                             \
        float e2 = EXP2(SX[2]), e3 = EXP2(SX[3]); LOADS; float e4 = EXP2(SX[4]), e5 = EXP2(SX[5]); SBAR(); \
        SY = NEXT; SBAR();                                                                              \
        float e6 = EXP2(SX[6]), e7 = EXP2(SX[7]);                                                       \
        u32x4 p0; p0[0] = cvtpk(e0, e1); p0[1] = cvtpk(e2, e3); p0[2] = cvtpk(e4, e5); p0[3] = cvtpk(e6, e7); SBAR(); \
        e0 = EXP2(SX[8]); e1 = EXP2(SX[9]); SBAR();                                                     \
        OX = MFMA(V0, BC8(p0), OX); SBAR();                                                             \
        e2 = EXP2(SX[10]); e3 = EXP2(SX[11]); e4 = EXP2(SX[12]); e5 = EXP2(SX[13]); e6 = EXP2(SX[14]); e7 = EXP2(SX[15]); \
        pp[0] = cvtpk(e0, e1); pp[1] = cvtpk(e2, e3); pp[2] = cvtpk(e4, e5); pp[3] = cvtpk(e6, e7); vq = V1; SBAR(); \
        e0 = EXP2(SY[0]); e1 = EXP2(SY[1]); SBAR();                                                     \
    } while (0)
#define ATT_TILE(KN, VN, EXTRA)                                                                         \
    do {                                                                                                \
        bf16x8 kn, n0, n1;                                                                              \
        ATT_HALF(sa, oa, ob, sb, MFMA(kf, qb, nmb), v0, v1, kn = lds_b128(KN); EXTRA);                  \
        ATT_HALF(sb, ob, oa, sa, MFMA(kn, qa, nma), v0, v1, (n0 = lds_b128(VN), n1 = lds_b128((VN) + 544))); \
        kf = kn; v0 = n0; v1 = n1;                                                                      \
    } while (0)
    __builtin_amdgcn_s_setprio(1);
    int cur = 0, nxt = A_SLOT, fre = 2 * A_SLOT;
#pragma unroll 1
    for (int c = 0; c < 8; ++c) {
        LAS const char* kc = L + cur + kln;
        LAS const char* vc = L + cur + vln;
        ATT_TILE(kc + 1 * 1024, vc + 1 * 1088, (void)0);
        ATT_TILE(kc + 2 * 1024, vc + 2 * 1088, KV_WRITE((LW + fre)));
        ATT_TILE(kc + 3 * 1024, vc + 3 * 1088, KV_LOAD(c + 3));
        ATT_TILE(kc + 4 * 1024, vc + 4 * 1088, (void)0);
        ATT_TILE(kc + 5 * 1024, vc + 5 * 1088, (void)0);
        ATT_TILE(kc + 6 * 1024, vc + 6 * 1088, (void)0);
        ATT_TILE(kc + 7 * 1024, vc + 7 * 1088, (void)0);
        ATT_TILE(L + nxt + kln, L + nxt + vln, (void)0);
        asm volatile("s_waitcnt lgkmcnt(0)\n\ts_barrier" ::: "memory");
        const int t_ = cur; cur = nxt; nxt = fre; fre = t_;
    }
    ob = MFMA(vq, BC8(pp), ob);
    STAMP(0, 7);
#undef ATT_TILE
#undef ATT_HALF
#undef KV_LOAD
#undef KV_WRITE
    {
        const float la = oa[8], lb = ob[8];
        const bool bad = !(la > 9.094947e-13f && la < 1.0995116e12f && lb > 9.094947e-13f && lb < 1.0995116e12f);
        if (__builtin_expect(__any(bad), 0)) { oa = attn_slow(rkv, qa, lane); ob = attn_slow(rkv, qb, lane); }
    }
    const __amdgpu_buffer_rsrc_t ro = __builtin_amdgcn_make_buffer_rsrc(
        ws + WS_ACT + ((size_t)(2 * it + rgl) * NKS + 4 * g + 2 * p) * 1024, 0, 2048, 0x00020000);
    __builtin_amdgcn_raw_buffer_store_b128(pack8(oa, 0, __builtin_amdgcn_rcpf(oa[8])), ro, lo16, 0, SC1);
    __builtin_amdgcn_raw_buffer_store_b128(pack8(ob, 0, __builtin_amdgcn_rcpf(ob[8])), ro, lo16 + 1024, 0, SC1);
    asm volatile("s_waitcnt vmcnt(0)\n\ts_barrier" ::: "memory");
    if (threadIdx.x == 0) set_flag(ws, SY_O + (size_t)(it * 8 + g) * FL, tag);
    STAMP(0, 8);
}

constexpr int G_W = 32768;
DI void g_gateup(const u16* __restrict__ x, const u16* __restrict__ wg, const u16* __restrict__ wu, char* __restrict__ ws,
                 char* smem, const int bid, const int rt, const int cg, const int w, const int lane, u64& tag) {
    const int wr = w & 1, wn = w >> 1;
    const int r = lane & 31, h = lane >> 5;
    const unsigned lds0 = (unsigned)(uintptr_t)smem;
    const unsigned lo16 = lane * 16;
    LAS const char* L = (LAS const char*)smem;
    const int prow = lane >> 3, pslot = lane & 7;
    const unsigned xoA = (unsigned)((32 * w + prow) * DM * 2 + ((pslot ^ (prow >> 1)) * 16));
    const unsigned xoB = (unsigned)((32 * w + 8 + prow) * DM * 2 + ((pslot ^ (4 + (prow >> 1))) * 16));
    const unsigned wo0 = (unsigned)((8 * w + prow) * DF * 2 + ((pslot ^ (((prow >> 1) & 1) << 2)) * 16));
    const unsigned wo1 = wo0 + 128;
    const char* xb = (const char*)(x + (size_t)rt * 128 * DM);
    const char* wA = (const char*)(wg + cg * 128);
    const char* wB = (const char*)(wu + cg * 128);
    const int rot = ROTG ? (rt + 2 * cg) & 7 : 0;
#define G_DMAX(j, i)                                                                                      \
    do {                                                                                                  \
        if ((j) < 8) glds16s(xb + (((j) + rot) & 7) * 128 + ((i) >> 1) * (16 * DM * 2), ((i) & 1) ? xoB : xoA,          \
                             lds0 + ((j) & 1) * 16384 + w * 4096 + (i) * 1024);                           \
    } while (0)
#define G_DMAW(u, i)                                                                                      \
    do {                                                                                                  \
        if ((u) < 16) glds16s((((i) >> 1) ? wB : wA) + (unsigned)(((u) + 2 * rot) & 15) * (32 * DF * 2), ((i) & 1) ? wo1 : wo0, \
                              lds0 + G_W + ((u) % 3) * 16384 + w * 4096 + (i) * 1024);                    \
    } while (0)
#define GDELAY 0
    for (int i_ = 0; i_ < GDELAY; ++i_) __builtin_amdgcn_s_sleep(8);
#pragma unroll
    for (int i = 0; i < 4; ++i) G_DMAX(0, i);
#pragma unroll
    for (int i = 0; i < 4; ++i) G_DMAW(0, i);
#pragma unroll
    for (int i = 0; i < 4; ++i) G_DMAW(1, i);
    tag = load_epoch(ws) + 1;

    const int ga = 2 * wr + wn, gb = 2 * wr + 1 - wn;
    const int fx = (r >> 1) & 7;
    const int blk = (lane >> 4) & 1, q4 = (lane & 15) >> 2, p4 = lane & 3;
    const int wl0 = G_W + h * 4096 + wn * 1024 + q4 * 128 + (((blk * 2 + (p4 >> 1)) ^ (((q4 >> 1) & 1) << 2)) * 16) + (p4 & 1) * 8;
    const int wl1 = wl0 ^ 64;
    const int c0 = wl0 + wr * 2048, c1 = wl1 + wr * 2048, d0 = wl0 + (1 - wr) * 2048, d1 = wl1 + (1 - wr) * 2048;
    const bf16x8 sel = make_sel(lane);
    f32x16 ac0a = {}, ac0b = {}, ac1a = {}, ac1b = {}, ad0a = {}, ad0b = {}, ad1a = {}, ad1b = {};
    f32x4 cs0 = {}, cs1 = {};
    bf16x8 xa[2], xbf[2], fc0[2], fc1[2], fd0[2], fd1[2];
#define G_XS(u, j) (L + (((u) >> 1) & 1) * 16384 + r * 128 + (((2 * (((u) & 1) * 2 + (j)) + h) ^ fx) * 16))
#define G_WO(u, j) (((u) % 3) * 16384 + (j) * 8192)
#define G_READ(set, u, j)                                                             \
    do {                                                                              \
        fc0[set] = lds_tr2<512>(L + c0 + G_WO(u, j));                                 \
        xa[set] = lds_b128(G_XS(u, j) + ga * 4096);                                   \
        xbf[set] = lds_b128(G_XS(u, j) + gb * 4096);                                  \
        fc1[set] = lds_tr2<512>(L + c1 + G_WO(u, j));                                 \
        fd0[set] = lds_tr2<512>(L + d0 + G_WO(u, j));                                 \
        fd1[set] = lds_tr2<512>(L + d1 + G_WO(u, j));                                 \
        SBAR();                                                                       \
    } while (0)
#define G_STEP(set, RD, nu, nj, D0, D1, D2, D3)                                       \
    do {                                                                              \
        ac0a = MFMA(fc0[set], xa[set], ac0a); SBAR();                                 \
        if (RD) { fc0[1 - set] = lds_tr2<512>(L + c0 + G_WO(nu, nj)); SBAR(); }       \
        ac0b = MFMA(fc0[set], xbf[set], ac0b); SBAR();                                \
        if (RD) { xa[1 - set] = lds_b128(G_XS(nu, nj) + ga * 4096); xbf[1 - set] = lds_b128(G_XS(nu, nj) + gb * 4096); SBAR(); } \
        ac1a = MFMA(fc1[set], xa[set], ac1a); SBAR();                                 \
        if (RD) { fc1[1 - set] = lds_tr2<512>(L + c1 + G_WO(nu, nj)); SBAR(); }       \
        ac1b = MFMA(fc1[set], xbf[set], ac1b); SBAR();                                \
        if (RD) { fd0[1 - set] = lds_tr2<512>(L + d0 + G_WO(nu, nj)); SBAR(); }       \
        ad0a = MFMA(fd0[set], xa[set], ad0a); SBAR();                                 \
        if (RD) { fd1[1 - set] = lds_tr2<512>(L + d1 + G_WO(nu, nj)); SBAR(); }       \
        ad0b = MFMA(fd0[set], xbf[set], ad0b); SBAR();                                \
        D0;                                                                           \
        ad1a = MFMA(fd1[set], xa[set], ad1a); SBAR();                                 \
        D1;                                                                           \
        ad1b = MFMA(fd1[set], xbf[set], ad1b); SBAR();                                \
        D2;                                                                           \
        cs0 = MFMA16(sel, fc0[set], cs0);                                             \
        cs1 = MFMA16(sel, fc1[set], cs1); SBAR();                                     \
        D3;                                                                           \
    } while (0)
    STAMP(1, 1);
    wait_bar<4>();
    STAMP(1, 2);
    if (lane == 0 && w == 1) set_flag(ws, SY_SEEN + (size_t)(256 + bid) * 8, tag);
    G_READ(0, 0, 0);
    G_DMAW(2, 0); G_DMAW(2, 1); G_DMAX(1, 0); G_DMAX(1, 1);
#pragma unroll
    for (int u = 0; u < 16; ++u) {
        if ((u & 1) == 0) G_STEP(0, true, u, 1, G_DMAW(u + 2, 2), G_DMAW(u + 2, 3), G_DMAX(u / 2 + 1, 2), G_DMAX(u / 2 + 1, 3));
        else              G_STEP(0, true, u, 1, G_DMAW(u + 2, 2), G_DMAW(u + 2, 3), (void)0, (void)0);
        if (u < 15) {
            const int nfly = (u + 2 < 16 ? 4 : 0) + ((((u & 1) == 0) && (u / 2 + 1 < 8)) ? 4 : 0);
            if (nfly == 8) wait_bar<8>(); else if (nfly == 4) wait_bar<4>(); else wait_bar<0>();
        }
        if (u & 1) G_STEP(1, u < 15, u + 1, 0, G_DMAW(u + 3, 0), G_DMAW(u + 3, 1), G_DMAX((u + 1) / 2 + 1, 0), G_DMAX((u + 1) / 2 + 1, 1));
        else       G_STEP(1, true, u + 1, 0, G_DMAW(u + 3, 0), G_DMAW(u + 3, 1), (void)0, (void)0);
    }
#undef G_STEP
#undef G_XS
#undef G_WO
#undef G_READ
#undef G_DMAX
#undef G_DMAW
    STAMP(1, 3);
    __builtin_amdgcn_s_setprio(2);
    float* st_cs = (float*)smem;
    if (h == 0) { st_cs[wr * 128 + wn * 64 + r] = cs0[0]; st_cs[wr * 128 + wn * 64 + 32 + r] = cs1[0]; }
    wait_flags(ws, (unsigned)(SY_KV + (rt * 4 + 2 * wr + (lane & 1)) * 2 * FL), tag);
    const __amdgpu_buffer_rsrc_t rst = __builtin_amdgcn_make_buffer_rsrc(ws + WS_STAT + rt * 1024, 0, 1024, 0x00020000);
    const u32x2 sta = __builtin_amdgcn_raw_buffer_load_b64(rst, ga * 256 + r * 8, 0, SC1);
    const u32x2 stb = __builtin_amdgcn_raw_buffer_load_b64(rst, gb * 256 + r * 8, 0, SC1);
    __syncthreads();
    const float mu_a = __uint_as_float(sta[0]), rs_a = __uint_as_float(sta[1]), mu_b = __uint_as_float(stb[0]), rs_b = __uint_as_float(stb[1]);
    {
        const bf16x8 ea = xext(mu_a, h), eb = xext(mu_b, h);
        const bf16x8 ec0 = wext(cs0[0], h), ec1 = wext(cs1[0], h);
        const bf16x8 ed0 = wext(st_cs[(1 - wr) * 128 + wn * 64 + r], h), ed1 = wext(st_cs[(1 - wr) * 128 + wn * 64 + 32 + r], h);
        ac0a = MFMA(ec0, ea, ac0a); ad0a = MFMA(ed0, ea, ad0a);
        ac0b = MFMA(ec0, eb, ac0b); ad0b = MFMA(ed0, eb, ad0b);
        ac1a = MFMA(ec1, ea, ac1a); ad1a = MFMA(ed1, ea, ad1a);
        ac1b = MFMA(ec1, eb, ac1b); ad1b = MFMA(ed1, eb, ad1b);
    }
    const __amdgpu_buffer_rsrc_t rh = __builtin_amdgcn_make_buffer_rsrc(
        ws + WS_ACT + ((size_t)rt * 4 * NKS + 32 + 8 * cg + 4 * wn) * 1024, 0, 4 * NKS * 1024, 0x00020000);
    const float K1c = 2.0f * 0.7978845608028654f * 1.4426950408889634f, K3c = K1c * 0.044715f;
    auto geglu = [&](const f32x16& G, const f32x16& U, float rs, int so) {
        const float c1_ = K1c * rs, c3_ = K3c * rs * rs * rs, ri2 = __builtin_amdgcn_rcpf(rs * rs);
        float hv[16];
#pragma unroll
        for (int i = 0; i < 16; ++i) {
            const float gg = G[i];
            const float t = gg * __builtin_fmaf(gg * gg, c3_, c1_);
            const float e = EXP2(-t);
            const float rr = __builtin_amdgcn_rcpf(__builtin_fmaf(e, ri2, ri2));
            hv[i] = (gg * U[i]) * rr;
        }
        u32x4 o0 = {cvtpk(hv[0], hv[1]), cvtpk(hv[2], hv[3]), cvtpk(hv[4], hv[5]), cvtpk(hv[6], hv[7])};
        u32x4 o1 = {cvtpk(hv[8], hv[9]), cvtpk(hv[10], hv[11]), cvtpk(hv[12], hv[13]), cvtpk(hv[14], hv[15])};
        __builtin_amdgcn_raw_buffer_store_b128(o0, rh, lo16 + so, 0, SC1);
        __builtin_amdgcn_raw_buffer_store_b128(o1, rh, lo16 + so + 1024, 0, SC1);
    };
    const int soa = ga * NKS * 1024, sob = gb * NKS * 1024;
    if (wr == 0) {
        geglu(ac0a, ad0a, rs_a, soa); geglu(ac0b, ad0b, rs_b, sob); geglu(ac1a, ad1a, rs_a, soa + 2048); geglu(ac1b, ad1b, rs_b, sob + 2048);
    } else {
        geglu(ad0a, ac0a, rs_a, soa); geglu(ad0b, ac0b, rs_b, sob); geglu(ad1a, ac1a, rs_a, soa + 2048); geglu(ad1b, ac1b, rs_b, sob + 2048);
    }
    asm volatile("s_waitcnt vmcnt(0) lgkmcnt(0)\n\ts_barrier" ::: "memory");
    if (threadIdx.x == 0) set_flag(ws, SY_H + (size_t)(rt * 16 + cg) * FL, tag);
}

DI void g_retile(const u16* __restrict__ wo, const u16* __restrict__ wd, char* __restrict__ ws, const int bid, const int j,
                 const int w, const int lane, u64& tag) {
    const int r = lane & 31, h = lane >> 5;
    const int ng = j >> 1, ks0 = 72 * (j & 1) + 18 * w;
    const __amdgpu_buffer_rsrc_t rw =
        __builtin_amdgcn_make_buffer_rsrc(ws + WS_W2F + ((size_t)ng * NKS + ks0) * 1024, 0, 18 * 1024, 0x00020000);
    tag = load_epoch(ws) + 1;
    if (lane == 0 && w == 1) set_flag(ws, SY_SEEN + (size_t)(256 + bid) * 8, tag);
#pragma unroll 1
    for (int b = 0; b < 3; ++b) {
        u16 v[6][8];
#pragma unroll
        for (int f = 0; f < 6; ++f) {
            const int ks = ks0 + b * 6 + f;
            const u16* src = (ks < 32 ? wo + (size_t)ks * 16 * DM : wd + (size_t)(ks - 32) * 16 * DM) + ng * 32 + r + h * 4 * DM;
#pragma unroll
            for (int e = 0; e < 8; ++e) v[f][e] = src[(8 * (e >> 2) + (e & 3)) * DM];
        }
#pragma unroll
        for (int f = 0; f < 6; ++f) {
            u32x4 o;
#pragma unroll
            for (int i = 0; i < 4; ++i) o[i] = (unsigned)v[f][2 * i] | ((unsigned)v[f][2 * i + 1] << 16);
            __builtin_amdgcn_raw_buffer_store_b128(o, rw, lane * 16 + (b * 6 + f) * 1024, 0, SC1);
        }
    }
    asm volatile("s_waitcnt vmcnt(0)\n\ts_barrier" ::: "memory");
    if (threadIdx.x == 0) set_flag(ws, SY_W + (size_t)j * FL, tag);
    if (j == 0 && w == 0) {
#pragma unroll 1
        for (int i = 0; i < 8; ++i) wait_flags(ws, SY_SEEN + i * 512 + lane * 8, tag);
        if (lane == 0) set_flag(ws, SY_EPOCH, tag);
    }
}

DI void role_g(const u16* __restrict__ x, const u16* __restrict__ wo, const u16* __restrict__ wg, const u16* __restrict__ wu,
               const u16* __restrict__ wd, char* __restrict__ ws, u16* __restrict__ out, char* smem, const int bid, const int w,
               const int lane) {
    const int xcd = bid & 7, idx = bid >> 3;
    const int rt = (xcd & 1) * 8 + (idx & 7), cg = (xcd >> 1) * 4 + (idx >> 3);
    const int it = xcd * 4 + (idx & 3), g = idx >> 2;
    const unsigned lo16 = lane * 16;
    u64 tag;
    __builtin_amdgcn_s_setprio(G1PRIO);
    STAMP(1, 0);
    if (cg < 14) g_gateup(x, wg, wu, ws, smem, bid, rt, cg, w, lane, tag);
    else g_retile(wo, wd, ws, bid, rt * 2 + (cg - 14), w, lane, tag);
    STAMP(1, 4);
    __builtin_amdgcn_s_setprio(0);

    wait_flags(ws, lane < 14 ? (unsigned)(SY_H + ((it >> 1) * 16 + lane) * FL) : (unsigned)(SY_W + (4 * g + (lane & 3)) * FL), tag);
    STAMP(1, 5);
    const __amdgpu_buffer_rsrc_t ra =
        __builtin_amdgcn_make_buffer_rsrc(ws + WS_ACT + (size_t)(2 * it) * NKS * 1024, 0, 2 * NKS * 1024, 0x00020000);
    const __amdgpu_buffer_rsrc_t rw =
        __builtin_amdgcn_make_buffer_rsrc(ws + WS_W2F + (size_t)(2 * g) * NKS * 1024, 0, 2 * NKS * 1024, 0x00020000);
    const int so = (32 + 28 * w) * 1024;
    constexpr int PD = 6;
    f32x16 acc00 = {}, acc01 = {}, acc10 = {}, acc11 = {};
    u32x4 fw0[8], fw1[8], fa0[8], fa1[8];
#define G_LOAD(i, k)                                                                                   \
    do {                                                                                               \
        fw0[i] = __builtin_amdgcn_raw_buffer_load_b128(rw, lo16, so + (k) * 1024, SC1);                \
        fa0[i] = __builtin_amdgcn_raw_buffer_load_b128(ra, lo16, so + (k) * 1024, SC1);                \
        fa1[i] = __builtin_amdgcn_raw_buffer_load_b128(ra, lo16, so + (NKS + (k)) * 1024, SC1);        \
        fw1[i] = __builtin_amdgcn_raw_buffer_load_b128(rw, lo16, so + (NKS + (k)) * 1024, SC1);        \
        SBAR();                                                                                        \
    } while (0)
#define G_MMA4(i)                                                                                      \
    do {                                                                                               \
        acc00 = MFMA(BC8(fw0[i]), BC8(fa0[i]), acc00); acc01 = MFMA(BC8(fw0[i]), BC8(fa1[i]), acc01);  \
        acc10 = MFMA(BC8(fw1[i]), BC8(fa0[i]), acc10); acc11 = MFMA(BC8(fw1[i]), BC8(fa1[i]), acc11);  \
        SBAR();                                                                                        \
    } while (0)
#pragma unroll
    for (int i = 0; i < PD; ++i) G_LOAD(i, i);
#pragma unroll
    for (int k = 0; k < 28; ++k) {
        G_MMA4(k % PD);
        if (k + PD < 28) G_LOAD(k % PD, k + PD);
    }
#pragma unroll
    for (int i = 0; i < 8; ++i) {
        fw0[i] = __builtin_amdgcn_raw_buffer_load_b128(rw, lo16, (8 * w + i) * 1024, SC1);
        fw1[i] = __builtin_amdgcn_raw_buffer_load_b128(rw, lo16, (NKS + 8 * w + i) * 1024, SC1);
    }
    u32x4 xv[2];
#pragma unroll
    for (int u = 0; u < 2; ++u) {
        const int t2 = threadIdx.x + 256 * u;
        xv[u] = *(const u32x4*)(x + (size_t)(it * 64 + (t2 >> 3)) * DM + g * 64 + (t2 & 7) * 8);
    }
    STAMP(1, 6);
    wait_flags(ws, (unsigned)(SY_O + (it * 8 + 2 * w + (lane & 1)) * FL), tag);
    STAMP(1, 7);
    __builtin_amdgcn_s_setprio(2);
#pragma unroll
    for (int i = 0; i < 8; ++i) {
        fa0[i] = __builtin_amdgcn_raw_buffer_load_b128(ra, lo16, (8 * w + i) * 1024, SC1);
        fa1[i] = __builtin_amdgcn_raw_buffer_load_b128(ra, lo16, (NKS + 8 * w + i) * 1024, SC1);
    }
    SBAR();
#pragma unroll
    for (int i = 0; i < 8; ++i) G_MMA4(i);
#undef G_LOAD
#undef G_MMA4
    char* dst = smem + w * 16640 + lo16;
#pragma unroll
    for (int q = 0; q < 4; ++q) {
        *(f32x4*)(dst + (0 + q) * 1040) = (f32x4){acc00[4 * q], acc00[4 * q + 1], acc00[4 * q + 2], acc00[4 * q + 3]};
        *(f32x4*)(dst + (4 + q) * 1040) = (f32x4){acc01[4 * q], acc01[4 * q + 1], acc01[4 * q + 2], acc01[4 * q + 3]};
        *(f32x4*)(dst + (8 + q) * 1040) = (f32x4){acc10[4 * q], acc10[4 * q + 1], acc10[4 * q + 2], acc10[4 * q + 3]};
        *(f32x4*)(dst + (12 + q) * 1040) = (f32x4){acc11[4 * q], acc11[4 * q + 1], acc11[4 * q + 2], acc11[4 * q + 3]};
    }
    STAMP(1, 8);
    __syncthreads();
#pragma unroll
    for (int u = 0; u < 2; ++u) {
        const int t2 = threadIdx.x + 256 * u, row = t2 >> 3, c8 = t2 & 7;
        const int a = c8 >> 2, q = c8 & 3, b = row >> 5, rr = row & 31;
        const char* s0 = smem + ((a * 2 + b) * 4 + q) * 1040 + rr * 16;
        f32x4 lo = {__uint_as_float(xv[u][0] << 16), __uint_as_float(xv[u][0] & 0xffff0000u), __uint_as_float(xv[u][1] << 16), __uint_as_float(xv[u][1] & 0xffff0000u)};
        f32x4 hi = {__uint_as_float(xv[u][2] << 16), __uint_as_float(xv[u][2] & 0xffff0000u), __uint_as_float(xv[u][3] << 16), __uint_as_float(xv[u][3] & 0xffff0000u)};
#pragma unroll
        for (int gw = 0; gw < 4; ++gw) {
            lo += *(const f32x4*)(s0 + gw * 16640);
            hi += *(const f32x4*)(s0 + gw * 16640 + 512);
        }
        u32x4 o = {cvtpk(lo[0], lo[1]), cvtpk(lo[2], lo[3]), cvtpk(hi[0], hi[1]), cvtpk(hi[2], hi[3])};
        *(u32x4*)(out + (size_t)(it * 64 + row) * DM + g * 64 + c8 * 8) = o;
    }
    STAMP(1, 9);
}

__global__ __launch_bounds__(256, 2) void fused_block(const u16* __restrict__ x, const u16* __restrict__ wq,
                                                      const u16* __restrict__ wk, const u16* __restrict__ wv,
                                                      const u16* __restrict__ wo, const u16* __restrict__ wg,
                                                      const u16* __restrict__ wu, const u16* __restrict__ wd,
                                                      char* __restrict__ ws, u16* __restrict__ out) {
    __shared__ __attribute__((aligned(16))) char smem[81920];
    const int lane = threadIdx.x & 63;
    const int w = __builtin_amdgcn_readfirstlane(threadIdx.x >> 6);
    if (blockIdx.x < 256) role_a(x, wq, wk, wv, ws, smem, blockIdx.x, w, lane);
    else role_g(x, wo, wg, wu, wd, ws, out, smem, blockIdx.x - 256, w, lane);
}

extern "C" void kernel_launch(void* const* d_in, const int* in_sizes, int n_in, void* d_out, int out_size, void* d_ws,
                              size_t ws_size, hipStream_t stream) {
    fused_block<<<512, 256, 0, stream>>>((const u16*)d_in[0], (const u16*)d_in[1], (const u16*)d_in[2], (const u16*)d_in[3],
                                         (const u16*)d_in[4], (const u16*)d_in[6], (const u16*)d_in[7], (const u16*)d_in[8],
                                         (char*)d_ws, (u16*)d_out);
}
```

```cpp
#include <hip/hip_runtime.h>
#include <hip/hip_bf16.h>
#include <stdint.h>

typedef __bf16 bf16x8 __attribute__((ext_vector_type(8)));
typedef __bf16 bf16x2 __attribute__((ext_vector_type(2)));
typedef short s16x4 __attribute__((ext_vector_type(4)));
typedef float f32x16 __attribute__((ext_vector_type(16)));
typedef float f32x4 __attribute__((ext_vector_type(4)));
typedef unsigned u32x4 __attribute__((ext_vector_type(4)));
typedef unsigned u32x2 __attribute__((ext_vector_type(2)));
typedef unsigned short u16;
typedef unsigned long long u64;
typedef __attribute__((address_space(1))) u64 gu64;
#define LAS __attribute__((address_space(3)))
#define DI __device__ __forceinline__
#define MFMA(a, b, c) __builtin_amdgcn_mfma_f32_32x32x16_bf16((a), (b), (c), 0, 0, 0)
#define MFMA16(a, b, c) __builtin_amdgcn_mfma_f32_16x16x32_bf16((a), (b), (c), 0, 0, 0)
#define SBAR() __builtin_amdgcn_sched_barrier(0)
#define EXP2(v) __builtin_amdgcn_exp2f(v)
#define BC8(v) __builtin_bit_cast(bf16x8, v)
#define SC1 16

constexpr int DM = 512, DF = 1792;
constexpr int NKS = 144;
constexpr size_t WS_KF = 0;
constexpr size_t WS_VF = 65536;
constexpr size_t WS_STAT = 131072;
constexpr size_t WS_SYNC = 131072 + 16384;
constexpr size_t SY_EPOCH = 0, SY_SEEN = 4096, SY_W = 8192, SY_KV = 16384, SY_H = 32768, SY_O = 65536;
constexpr int FL = 128;
constexpr size_t WS_ACT = 262144;
constexpr size_t WS_W2F = WS_ACT + (size_t)64 * NKS * 1024;
constexpr size_t WS_END = WS_W2F + (size_t)16 * NKS * 1024;
constexpr unsigned SPIN_CAP = 1u << 18;
#define G1PRIO 0
#define ROTG 0

#define STAMP(kern, i)

DI unsigned cvtpk(float lo, float hi) {
    bf16x2 r = {(__bf16)lo, (__bf16)hi};
    return __builtin_bit_cast(unsigned, r);
}
DI float bf_round(float v) { return (float)(__bf16)v; }
DI float swap_add(float v) {
    auto rr = __builtin_amdgcn_permlane32_swap(__float_as_uint(v), __float_as_uint(v), false, false);
    return __uint_as_float(rr[0]) + __uint_as_float(rr[1]);
}
DI float swap_max(float v) {
    auto rr = __builtin_amdgcn_permlane32_swap(__float_as_uint(v), __float_as_uint(v), false, false);
    return __builtin_fmaxf(__uint_as_float(rr[0]), __uint_as_float(rr[1]));
}
DI void glds16s(const void* sbase, unsigned voff, unsigned lds_base) {
    asm volatile("s_mov_b32 m0, %2\n\ts_nop 0\n\tglobal_load_lds_dwordx4 %0, %1" ::"v"(voff), "s"(sbase), "s"(lds_base) : "memory");
    SBAR();
}
template <int N> DI void wait_bar() { asm volatile("s_waitcnt vmcnt(%0) lgkmcnt(0)\n\ts_barrier" ::"n"(N) : "memory"); }
DI bf16x8 lds_b128(LAS const char* p) { return *(LAS const bf16x8*)p; }
template <int STEP> DI bf16x8 lds_tr2(LAS const char* p) {
    s16x4 lo = __builtin_amdgcn_ds_read_tr16_b64_v4i16((LAS s16x4*)p);
    s16x4 hi = __builtin_amdgcn_ds_read_tr16_b64_v4i16((LAS s16x4*)(p + STEP));
    return __builtin_bit_cast(bf16x8, __builtin_shufflevector(lo, hi, 0, 1, 2, 3, 4, 5, 6, 7));
}
DI u32x4 pack8(const f32x16& v, int o, float s) {
    u32x4 r;
#pragma unroll
    for (int i = 0; i < 4; ++i) r[i] = cvtpk(v[o + 2 * i] * s, v[o + 2 * i + 1] * s);
    return r;
}
DI void ln_stat(float sum, float sq, float& mu, float& rs) {
    const float mean = sum * (1.0f / DM);
    const float var = __builtin_fmaxf(sq * (1.0f / DM) - mean * mean, 0.f);
    const float sd = bf_round(__builtin_amdgcn_sqrtf(bf_round(bf_round(var) + 1e-5f)));
    mu = bf_round(mean); rs = __builtin_amdgcn_rcpf(sd);
}
DI bf16x8 wext(float cs, int h) {
    const float hi = bf_round(cs);
    u32x4 v = {h == 0 ? cvtpk(hi, cs - hi) : 0u, 0u, 0u, 0u};
    return BC8(v);
}
DI bf16x8 xext(float mu, int h) {
    u32x4 v = {h == 0 ? cvtpk(-mu, -mu) : 0u, 0u, 0u, 0u};
    return BC8(v);
}
DI bf16x8 make_sel(int lane) {
    const unsigned o1 = ((lane & 3) == 0 && ((lane >> 2) & 1) == ((lane >> 4) & 1)) ? 0x3F803F80u : 0u;
    return BC8(((u32x4){o1, o1, o1, o1}));
}
DI float diag32(const f32x16& a, int r) {
    typedef float f32x8 __attribute__((ext_vector_type(8)));
    typedef float f32x2 __attribute__((ext_vector_type(2)));
    const f32x8 t8 = (r & 16) ? __builtin_shufflevector(a, a, 8, 9, 10, 11, 12, 13, 14, 15)
                              : __builtin_shufflevector(a, a, 0, 1, 2, 3, 4, 5, 6, 7);
    const f32x4 t4 = (r & 8) ? __builtin_shufflevector(t8, t8, 4, 5, 6, 7) : __builtin_shufflevector(t8, t8, 0, 1, 2, 3);
    const f32x2 t2 = (r & 2) ? __builtin_shufflevector(t4, t4, 2, 3) : __builtin_shufflevector(t4, t4, 0, 1);
    const float t20 = t2[0], t21 = t2[1];
    return (r & 1) ? t21 : t20;
}
DI u64 load_epoch(const char* ws) {
    u64 e;
    asm volatile("s_load_dwordx2 %0, %1, 0x0\n\ts_waitcnt lgkmcnt(0)" : "=s"(e) : "s"(ws + WS_SYNC + SY_EPOCH) : "memory");
    return e;
}
DI void set_flag(char* ws, size_t off, u64 tag) {
    __hip_atomic_store((gu64*)(ws + WS_SYNC + off), tag, __ATOMIC_RELAXED, __HIP_MEMORY_SCOPE_AGENT);
}
DI void wait_flags(char* ws, unsigned off, u64 tag) {
    gu64* p = (gu64*)(ws + WS_SYNC + off);
    for (unsigned spins = 0; spins < SPIN_CAP; ++spins) {
        const u64 v = __hip_atomic_load(p, __ATOMIC_RELAXED, __HIP_MEMORY_SCOPE_AGENT);
        if (__all(v == tag)) break;
        __builtin_amdgcn_s_sleep(2);
    }
    __builtin_amdgcn_fence(__ATOMIC_ACQUIRE, "wavefront");
    asm volatile("" ::: "memory");
}
DI float max16(const f32x16& s) {
    float a = __builtin_fmaxf(s[0], s[1]);
#pragma unroll
    for (int i = 2; i < 16; i += 2) a = __builtin_fmaxf(__builtin_fmaxf(a, s[i]), s[i + 1]);
    return a;
}

DI f32x16 attn_slow(const __amdgpu_buffer_rsrc_t rkv, const bf16x8 q, const int lane) {
    const int r = lane & 31, h = lane >> 5;
    const unsigned vo = (unsigned)WS_VF + h * 256 + (r & 15) * 16;
    float m = -3.0e38f;
    f32x16 o = {};
#pragma unroll 1
    for (int t = 0; t < 64; ++t) {
        const u32x4 kf = __builtin_amdgcn_raw_buffer_load_b128(rkv, lane * 16, t * 1024, SC1);
        u32x4 v0 = __builtin_amdgcn_raw_buffer_load_b128(rkv, vo, t * 1024, SC1);
        u32x4 v1 = __builtin_amdgcn_raw_buffer_load_b128(rkv, vo + 512, t * 1024, SC1);
        if (r >= 16) { v0 = (u32x4){0x3F803F80u, 0x3F803F80u, 0x3F803F80u, 0x3F803F80u}; v1 = v0; }
        f32x16 z = {};
        f32x16 s = MFMA(BC8(kf), q, z);
        const float mn = __builtin_fmaxf(m, swap_max(max16(s)));
        const float al = EXP2(m - mn);
        m = mn;
#pragma unroll
        for (int i = 0; i < 16; ++i) { s[i] -= mn; o[i] *= al; }
        u32x4 p0, p1;
#pragma unroll
        for (int i = 0; i < 4; ++i) {
            p0[i] = cvtpk(EXP2(s[2 * i]), EXP2(s[2 * i + 1]));
            p1[i] = cvtpk(EXP2(s[8 + 2 * i]), EXP2(s[8 + 2 * i + 1]));
        }
        o = MFMA(BC8(v0), BC8(p0), o);
        o = MFMA(BC8(v1), BC8(p1), o);
    }
    return o;
}

constexpr int A_STAGE = 16384;
constexpr int A_SLOT = 8 * 1024 + 8 * 1088;
DI void role_a(const u16* __restrict__ x, const u16* __restrict__ wq, char* __restrict__ ws, char* smem, const int bid, const int w, const int lane) {
    const int xcd = bid & 7, idx = bid >> 3;
    const int it = xcd * 4 + (idx & 3), g = idx >> 2;
    const int p = w >> 1, rgl = w & 1;
    const int r = lane & 31, h = lane >> 5;
    const unsigned lds0 = (unsigned)(uintptr_t)smem;
    const unsigned lo16 = lane * 16;
    LAS const char* L = (LAS const char*)smem;
    __builtin_amdgcn_s_setprio(2);
    STAMP(0, 0);

    const int prow = lane >> 3, pslot = lane & 7;
    const unsigned xo0 = (unsigned)((16 * w + prow) * DM * 2 + ((pslot ^ (((16 * w + prow) >> 1) & 7)) * 16));
    const unsigned xo1 = (unsigned)((16 * w + 8 + prow) * DM * 2 + ((pslot ^ (((16 * w + 8 + prow) >> 1) & 7)) * 16));
    const unsigned wo0 = (unsigned)((16 * w + prow) * DM * 2 + ((pslot ^ (((prow >> 1) & 1) << 2)) * 16));
    const unsigned wo1 = wo0 + 8 * DM * 2;
    const char* xb = (const char*)(x + (size_t)it * 64 * DM);
    const char* wb = (const char*)(wq + g * 64);
    const int blk = (lane >> 4) & 1, q4 = (lane & 15) >> 2, p4 = lane & 3;
    const bf16x8 sel = make_sel(lane);
#define A_DMA(s, i)                                                                                       \
    do {                                                                                                  \
        if ((s) < 8) {                                                                                    \
            const unsigned sb_ = lds0 + ((s) & 3) * A_STAGE + w * 2048;                                   \
            if ((i) == 0) glds16s(xb + (s) * 128, xo0, sb_);                                              \
            if ((i) == 1) glds16s(xb + (s) * 128, xo1, sb_ + 1024);                                       \
            if ((i) == 2) glds16s(wb + (s) * (64 * DM * 2), wo0, sb_ + 8192);                             \
            if ((i) == 3) glds16s(wb + (s) * (64 * DM * 2), wo1, sb_ + 8192 + 1024);                      \
        }                                                                                                 \
    } while (0)
#define A_DMA4(s) do { A_DMA(s, 0); A_DMA(s, 1); A_DMA(s, 2); A_DMA(s, 3); } while (0)
    A_DMA4(0); A_DMA4(1); A_DMA4(2); A_DMA4(3);
    const u64 tag = load_epoch(ws) + 1;

    const int fx = ((r >> 1) & 7) ^ (p << 1);
    const int wlane = 8192 + h * 1024 + q4 * 128 + (((p * 4 + blk * 2 + (p4 >> 1)) ^ (((q4 >> 1) & 1) << 2)) * 16) + (p4 & 1) * 8;
    f32x16 accq = {}, accx = {};
    f32x4 acc_cs = {}, acc_rs = {};
    bf16x8 xa[2], wc[2];
#define A_READ(set, s, j)                                                             \
    do {                                                                              \
        LAS const char* sb_ = L + ((s) & 3) * A_STAGE;                                \
        const int ks_ = (j) + (((j) & 1) ? -p : p);                                   \
        wc[set] = lds_tr2<512>(sb_ + wlane + ks_ * 2048);                             \
        xa[set] = lds_b128(sb_ + (rgl * 32 + r) * 128 + (((2 * (j) + h) ^ fx) * 16)); \
        SBAR();                                                                       \
    } while (0)
#define A_MMA(set, j, ds, d0)                                                         \
    do {                                                                              \
        accq = MFMA(wc[set], xa[set], accq);                                          \
        acc_cs = MFMA16(sel, wc[set], acc_cs);                                        \
        if (((j) & 1) == 0) {                                                         \
            accx = MFMA(xa[set], xa[set], accx);                                      \
            acc_rs = MFMA16(sel, xa[set], acc_rs);                                    \
        }                                                                             \
        SBAR();                                                                       \
        A_DMA(ds, d0);                                                                \
    } while (0)
    STAMP(0, 1);
    wait_bar<12>();
    STAMP(0, 2);
    if (lane == 0 && w == 1) set_flag(ws, SY_SEEN + (size_t)bid * 8, tag);
    A_READ(0, 0, 0);
#pragma unroll
    for (int s = 0; s < 8; ++s) {
        A_READ(1, s, 1); A_MMA(0, 0, (s >= 1 ? s + 3 : 8), 1);
        A_READ(0, s, 2); A_MMA(1, 1, (s >= 1 ? s + 3 : 8), 2);
        A_READ(1, s, 3); A_MMA(0, 2, (s >= 1 ? s + 3 : 8), 3);
        if (s < 7) {
            if (s <= 4) wait_bar<8>(); else if (s == 5) wait_bar<4>(); else wait_bar<0>();
            A_READ(0, s + 1, 0);
        }
        A_MMA(1, 3, s + 4, 0);
    }
#undef A_READ
#undef A_MMA
#undef A_DMA
#undef A_DMA4
    STAMP(0, 3);
    bf16x8 qa, qb;
    {
        const float dg = diag32(accx, r);
        float* st = (float*)(smem + 65536);
        if (h == ((r >> 2) & 1)) { st[p * 64 + rgl * 32 + r] = acc_rs[0]; st[128 + p * 64 + rgl * 32 + r] = dg; }
        __syncthreads();
        float mu, rs;
        ln_stat(st[rgl * 32 + r] + st[64 + rgl * 32 + r], st[128 + rgl * 32 + r] + st[192 + rgl * 32 + r], mu, rs);
        accq = MFMA(wext(acc_cs[0], h), xext(mu, h), accq);
        const float qs = rs * (0.25f * 1.4426950408889634f);
        qa = BC8(pack8(accq, 0, qs));
        qb = BC8(pack8(accq, 8, qs));
    }
    STAMP(0, 4);

    const __amdgpu_buffer_rsrc_t rkv = __builtin_amdgcn_make_buffer_rsrc(ws, 0, 131072, 0x00020000);
    gu64* kvf = (gu64*)(ws + WS_SYNC + SY_KV + (size_t)((((lane >> 2) & 7) * 8 + 2 * w) * 2 + (lane & 3)) * FL);
    unsigned long long rdy = 0;
    for (unsigned spins = 0; spins < SPIN_CAP; ++spins) {
        rdy = __ballot(__hip_atomic_load(kvf, __ATOMIC_RELAXED, __HIP_MEMORY_SCOPE_AGENT) == tag);
        if ((rdy & 0xFFFull) == 0xFFFull) break;
        __builtin_amdgcn_s_sleep(1);
    }
    __builtin_amdgcn_fence(__ATOMIC_ACQUIRE, "wavefront");
    asm volatile("" ::: "memory");
    STAMP(0, 5);
    u32x4 sk0, sk1, sv0, sv1;
#define KV_LOAD(c)                                                                                     \
    do {                                                                                               \
        const int so_ = ((c) * 8 + 2 * w) * 1024;                                                      \
        sk0 = __builtin_amdgcn_raw_buffer_load_b128(rkv, lo16, so_, SC1);                              \
        sk1 = __builtin_amdgcn_raw_buffer_load_b128(rkv, lo16 + 1024, so_, SC1);                       \
        sv0 = __builtin_amdgcn_raw_buffer_load_b128(rkv, lo16 + (unsigned)WS_VF, so_, SC1);            \
        sv1 = __builtin_amdgcn_raw_buffer_load_b128(rkv, lo16 + (unsigned)WS_VF + 1024, so_, SC1);     \
    } while (0)
    const int kwr = w * 2048 + lo16, vwr = 8192 + w * 2176 + (lane >> 4) * 272 + (lane & 15) * 16;
#define KV_WRITE(slotbase)                                                                             \
    do {                                                                                               \
        *(LAS u32x4*)(slotbase + kwr) = sk0; *(LAS u32x4*)(slotbase + kwr + 1024) = sk1;               \
        *(LAS u32x4*)(slotbase + vwr) = sv0; *(LAS u32x4*)(slotbase + vwr + 1088) = sv1;               \
    } while (0)
    LAS char* LW = (LAS char*)smem;
    {
        u32x4 tk0, tk1, tv0, tv1, uk0, uk1, uv0, uv1;
        KV_LOAD(0);
        tk0 = sk0; tk1 = sk1; tv0 = sv0; tv1 = sv1;
        KV_LOAD(1);
        uk0 = sk0; uk1 = sk1; uv0 = sv0; uv1 = sv1;
        KV_LOAD(2);
        if (threadIdx.x < 96) {
            const int s3 = threadIdx.x >> 5, e = threadIdx.x & 31;
            *(LAS u32x4*)(LW + s3 * A_SLOT + 8192 + e * 272 + 256) = (u32x4){0x3F803F80u, 0x3F803F80u, 0x3F803F80u, 0x3F803F80u};
        }
        *(LAS u32x4*)(LW + kwr) = tk0; *(LAS u32x4*)(LW + kwr + 1024) = tk1;
        *(LAS u32x4*)(LW + vwr) = tv0; *(LAS u32x4*)(LW + vwr + 1088) = tv1;
        *(LAS u32x4*)(LW + A_SLOT + kwr) = uk0; *(LAS u32x4*)(LW + A_SLOT + kwr + 1024) = uk1;
        *(LAS u32x4*)(LW + A_SLOT + vwr) = uv0; *(LAS u32x4*)(LW + A_SLOT + vwr + 1088) = uv1;
    }
    asm volatile("s_waitcnt lgkmcnt(0)\n\ts_barrier" ::: "memory");
    STAMP(0, 6);
    const int kln = lo16, vln = 8192 + h * 272 + (r < 16 ? r : 16) * 16;
    f32x16 nma, nmb;
    bf16x8 kf = lds_b128(L + kln);
    bf16x8 v0 = lds_b128(L + vln), v1 = lds_b128(L + vln + 544);
    {
        f32x16 z = {};
        f32x16 s0 = MFMA(kf, qa, z), s1 = MFMA(kf, qb, z);
        const float ma = swap_max(max16(s0)), mb = swap_max(max16(s1));
#pragma unroll
        for (int i = 0; i < 16; ++i) { nma[i] = -ma; nmb[i] = -mb; }
    }
    f32x16 oa = {}, ob = {};
    f32x16 sa = MFMA(kf, qa, nma), sb;
    float e0, e1;
    u32x4 pp = {0u, 0u, 0u, 0u};
    bf16x8 vq = v1;
    e0 = EXP2(sa[0]); e1 = EXP2(sa[1]);
#define ATT_HALF(SX, OX, OP, SY, NEXT, V0, V1, LOADS)                                                   \
    do {                                                                                                \
        OP = MFMA(vq, BC8(pp), OP); SBAR();                                                             \
        float e2 = EXP2(SX[2]), e3 = EXP2(SX[3]); LOADS; float e4 = EXP2(SX[4]), e5 = EXP2(SX[5]); SBAR(); \
        SY = NEXT; SBAR();                                                                              \
        float e6 = EXP2(SX[6]), e7 = EXP2(SX[7]);                                                       \
        u32x4 p0; p0[0] = cvtpk(e0, e1); p0[1] = cvtpk(e2, e3); p0[2] = cvtpk(e4, e5); p0[3] = cvtpk(e6, e7); SBAR(); \
        e0 = EXP2(SX[8]); e1 = EXP2(SX[9]); SBAR();                                                     \
        OX = MFMA(V0, BC8(p0), OX); SBAR();                                                             \
        e2 = EXP2(SX[10]); e3 = EXP2(SX[11]); e4 = EXP2(SX[12]); e5 = EXP2(SX[13]); e6 = EXP2(SX[14]); e7 = EXP2(SX[15]); \
        pp[0] = cvtpk(e0, e1); pp[1] = cvtpk(e2, e3); pp[2] = cvtpk(e4, e5); pp[3] = cvtpk(e6, e7); vq = V1; SBAR(); \
        e0 = EXP2(SY[0]); e1 = EXP2(SY[1]); SBAR();                                                     \
    } while (0)
#define ATT_TILE(KN, VN, EXTRA)                                                                         \
    do {                                                                                                \
        bf16x8 kn, n0, n1;                                                                              \
        ATT_HALF(sa, oa, ob, sb, MFMA(kf, qb, nmb), v0, v1, kn = lds_b128(KN); EXTRA);                  \
        ATT_HALF(sb, ob, oa, sa, MFMA(kn, qa, nma), v0, v1, (n0 = lds_b128(VN), n1 = lds_b128((VN) + 544))); \
        kf = kn; v0 = n0; v1 = n1;                                                                      \
    } while (0)
    __builtin_amdgcn_s_setprio(1);
    int cur = 0, nxt = A_SLOT, fre = 2 * A_SLOT;
#pragma unroll 1
    for (int c = 0; c < 8; ++c) {
        LAS const char* kc = L + cur + kln;
        LAS const char* vc = L + cur + vln;
        u64 fv = tag;
        if (rdy != ~0ull) fv = __hip_atomic_load(kvf, __ATOMIC_RELAXED, __HIP_MEMORY_SCOPE_AGENT);
        SBAR();
        ATT_TILE(kc + 1 * 1024, vc + 1 * 1088, (void)0);
        ATT_TILE(kc + 2 * 1024, vc + 2 * 1088, KV_WRITE((LW + fre)));
        if (rdy != ~0ull) {
            rdy = __ballot(fv == tag);
            const unsigned long long need = c < 5 ? 0xFull << (4 * (c + 3)) : 0ull;
            for (unsigned spins = 0; (rdy & need) != need && spins < SPIN_CAP; ++spins)
                rdy = __ballot(__hip_atomic_load(kvf, __ATOMIC_RELAXED, __HIP_MEMORY_SCOPE_AGENT) == tag);
            __builtin_amdgcn_fence(__ATOMIC_ACQUIRE, "wavefront");
            asm volatile("" ::: "memory");
        }
        SBAR();
        ATT_TILE(kc + 3 * 1024, vc + 3 * 1088, KV_LOAD(c + 3));
        ATT_TILE(kc + 4 * 1024, vc + 4 * 1088, (void)0);
        ATT_TILE(kc + 5 * 1024, vc + 5 * 1088, (void)0);
        ATT_TILE(kc + 6 * 1024, vc + 6 * 1088, (void)0);
        ATT_TILE(kc + 7 * 1024, vc + 7 * 1088, (void)0);
        ATT_TILE(L + nxt + kln, L + nxt + vln, (void)0);
        asm volatile("s_waitcnt lgkmcnt(0)\n\ts_barrier" ::: "memory");
        const int t_ = cur; cur = nxt; nxt = fre; fre = t_;
    }
    ob = MFMA(vq, BC8(pp), ob);
    STAMP(0, 7);
#undef ATT_TILE
#undef ATT_HALF
#undef KV_LOAD
#undef KV_WRITE
    {
        const float la = oa[8], lb = ob[8];
        const bool bad = !(la > 9.094947e-13f && la < 1.0995116e12f && lb > 9.094947e-13f && lb < 1.0995116e12f);
        if (__builtin_expect(__any(bad), 0)) { oa = attn_slow(rkv, qa, lane); ob = attn_slow(rkv, qb, lane); }
    }
    const __amdgpu_buffer_rsrc_t ro = __builtin_amdgcn_make_buffer_rsrc(
        ws + WS_ACT + ((size_t)(2 * it + rgl) * NKS + 4 * g + 2 * p) * 1024, 0, 2048, 0x00020000);
    __builtin_amdgcn_raw_buffer_store_b128(pack8(oa, 0, __builtin_amdgcn_rcpf(oa[8])), ro, lo16, 0, SC1);
    __builtin_amdgcn_raw_buffer_store_b128(pack8(ob, 0, __builtin_amdgcn_rcpf(ob[8])), ro, lo16 + 1024, 0, SC1);
    asm volatile("s_waitcnt vmcnt(0)\n\ts_barrier" ::: "memory");
    if (threadIdx.x == 0) set_flag(ws, SY_O + (size_t)(it * 8 + g) * FL, tag);
    STAMP(0, 8);
}

constexpr int J_X = 49152, J_W = 16384;
DI void kv_issue(const u16* __restrict__ x, const u16* __restrict__ wkv, char* smem, const int job, const int w, const int lane) {
    const unsigned lds0 = (unsigned)(uintptr_t)smem;
    const int prow = lane >> 3, pslot = lane & 7;
    const unsigned joA = (unsigned)(prow * DM * 2 + ((pslot ^ (prow >> 1)) * 16));
    const unsigned joB = (unsigned)((8 + prow) * DM * 2 + ((pslot ^ (4 + (prow >> 1))) * 16));
    const char* xj = (const char*)(x + (size_t)(job >> 1) * 32 * DM) + w * 256;
    const char* kvw = (const char*)wkv + w * 4096;
#pragma unroll
    for (int sl = 0; sl < 2; ++sl) {
#pragma unroll
        for (int i = 0; i < 4; ++i)
            glds16s(xj + sl * 128 + (i >> 1) * (16 * DM * 2), (i & 1) ? joB : joA, lds0 + J_X + w * 8192 + sl * 4096 + i * 1024);
        glds16s(kvw + sl * 2048, lane * 16, lds0 + J_W + w * 4096 + sl * 2048);
        glds16s(kvw + sl * 2048 + 1024, lane * 16, lds0 + J_W + w * 4096 + sl * 2048 + 1024);
    }
}
template <int YOUNG>
DI void kv_job(char* __restrict__ ws, char* smem, const int job, const int w, const int lane) {
    const int r = lane & 31, h = lane >> 5, q4 = (lane & 15) >> 2, p4 = lane & 3;
    const int rg = job >> 1;
    const bool isv = job & 1;
    const unsigned lo16 = lane * 16;
    LAS const char* L = (LAS const char*)smem;
    const bf16x8 sel = make_sel(lane);
    LAS const char* xl = L + J_X + w * 8192 + r * 128;
    LAS const char* kl = L + J_W + w * 4096 + (8 * h + q4) * 32 + p4 * 8;
    const int fj = (r >> 1) & 7;
    f32x16 acck = {}, accx = {};
    f32x4 acc_ck = {}, acc_rs = {};
    bf16x8 xf[8], kf[8];
    asm volatile("s_waitcnt vmcnt(%0)" ::"n"(6 + YOUNG) : "memory");
#pragma unroll
    for (int k = 0; k < 4; ++k) { xf[k] = lds_b128(xl + (((2 * k + h) ^ fj) * 16)); kf[k] = lds_tr2<128>(kl + k * 512); }
    asm volatile("s_waitcnt vmcnt(%0)" ::"n"(YOUNG) : "memory");
#pragma unroll
    for (int k = 4; k < 8; ++k) { xf[k] = lds_b128(xl + 4096 + (((2 * (k - 4) + h) ^ fj) * 16)); kf[k] = lds_tr2<128>(kl + k * 512); }
    if (isv) {
#pragma unroll
        for (int k = 0; k < 8; ++k) acck = MFMA(xf[k], kf[k], acck);
    } else {
#pragma unroll
        for (int k = 0; k < 8; ++k) acck = MFMA(kf[k], xf[k], acck);
    }
#pragma unroll
    for (int k = 0; k < 8; ++k) {
        accx = MFMA(xf[k], xf[k], accx);
        acc_rs = MFMA16(sel, xf[k], acc_rs);
        acc_ck = MFMA16(sel, kf[k], acc_ck);
    }
    char* pq = smem + J_W + w * 4096;
    if (isv) {
        if (r < 16) {
#pragma unroll
            for (int q = 0; q < 4; ++q)
                *(f32x4*)(pq + q * 512 + (h * 16 + r) * 16) = (f32x4){acck[4 * q], acck[4 * q + 1], acck[4 * q + 2], acck[4 * q + 3]};
        }
    } else {
        *(f32x4*)(pq + lo16) = (f32x4){acck[0], acck[1], acck[2], acck[3]};
        *(f32x4*)(pq + 1024 + lo16) = (f32x4){acck[4], acck[5], acck[6], acck[7]};
    }
    const float dg = diag32(accx, r);
    if (h == ((r >> 2) & 1)) { ((float*)(pq + 2048))[r] = acc_rs[0]; ((float*)(pq + 2176))[r] = dg; }
    if (lane < 16) ((float*)(pq + 2304))[lane] = acc_ck[0];
    asm volatile("s_waitcnt lgkmcnt(0)\n\ts_barrier" ::: "memory");
    if (w == 0) {
        const char* p0 = smem + J_W;
        float sum = 0.f, sq = 0.f, ck = 0.f;
#pragma unroll
        for (int v = 0; v < 4; ++v) {
            sum += ((const float*)(p0 + v * 4096 + 2048))[r];
            sq += ((const float*)(p0 + v * 4096 + 2176))[r];
            ck += ((const float*)(p0 + v * 4096 + 2304))[lane & 15];
        }
        float mu, rs;
        ln_stat(sum, sq, mu, rs);
        const bf16x8 ex = xext(mu, h), ek = wext(ck, h);
        const __amdgpu_buffer_rsrc_t rkv_st = __builtin_amdgcn_make_buffer_rsrc(ws, 0, 131072 + 16384, 0x00020000);
        if (isv) {
            float* srs = (float*)(smem + J_W + 2432);
            if (h == 0) srs[r] = rs;
#pragma unroll
            for (int v = 1; v < 4; ++v)
#pragma unroll
                for (int q = 0; q < 4; ++q) {
                    const f32x4 o = *(const f32x4*)(p0 + v * 4096 + q * 512 + (h * 16 + (r & 15)) * 16);
                    acck[4 * q] += o[0]; acck[4 * q + 1] += o[1]; acck[4 * q + 2] += o[2]; acck[4 * q + 3] += o[3];
                }
            acck = MFMA(ex, ek, acck);
            f32x16 v;
#pragma unroll
            for (int q = 0; q < 4; ++q) {
                const f32x4 rr = *(const f32x4*)(srs + 8 * q + 4 * h);
#pragma unroll
                for (int i = 0; i < 4; ++i) v[4 * q + i] = acck[4 * q + i] * rr[i];
            }
            const unsigned vo = r < 16 ? (unsigned)WS_VF + h * 256 + r * 16 : 0x40000000u;
            __builtin_amdgcn_raw_buffer_store_b128(pack8(v, 0, 1.0f), rkv_st, vo + rg * 1024, 0, SC1);
            __builtin_amdgcn_raw_buffer_store_b128(pack8(v, 8, 1.0f), rkv_st, vo + rg * 1024 + 512, 0, SC1);
        } else {
#pragma unroll
            for (int v = 1; v < 4; ++v)
#pragma unroll
                for (int q = 0; q < 2; ++q) {
                    const f32x4 o = *(const f32x4*)(p0 + v * 4096 + q * 1024 + lo16);
                    acck[4 * q] += o[0]; acck[4 * q + 1] += o[1]; acck[4 * q + 2] += o[2]; acck[4 * q + 3] += o[3];
                }
            acck = MFMA(ek, ex, acck);
            u32x2 ms = {__float_as_uint(mu), __float_as_uint(rs)};
            __builtin_amdgcn_raw_buffer_store_b64(ms, rkv_st, h == 0 ? (unsigned)WS_STAT + rg * 256 + r * 8 : 0x40000000u, 0, SC1);
            __builtin_amdgcn_raw_buffer_store_b128(pack8(acck, 0, rs), rkv_st, lo16 + rg * 1024, 0, SC1);
        }
    }
}

constexpr int G_W = 32768;
DI void g_gateup(const u16* __restrict__ x, const u16* __restrict__ wg, const u16* __restrict__ wu, char* __restrict__ ws,
                 char* smem, const int bid, const int rt, const int cg, const int job, const int w, const int lane, u64& tag) {
    const int wr = w & 1, wn = w >> 1;
    const int r = lane & 31, h = lane >> 5;
    const unsigned lds0 = (unsigned)(uintptr_t)smem;
    const unsigned lo16 = lane * 16;
    LAS const char* L = (LAS const char*)smem;
    const int prow = lane >> 3, pslot = lane & 7;
    const unsigned xoA = (unsigned)((32 * w + prow) * DM * 2 + ((pslot ^ (prow >> 1)) * 16));
    const unsigned xoB = (unsigned)((32 * w + 8 + prow) * DM * 2 + ((pslot ^ (4 + (prow >> 1))) * 16));
    const unsigned wo0 = (unsigned)((8 * w + prow) * DF * 2 + ((pslot ^ (((prow >> 1) & 1) << 2)) * 16));
    const unsigned wo1 = wo0 + 128;
    const char* xb = (const char*)(x + (size_t)rt * 128 * DM);
    const char* wA = (const char*)(wg + cg * 128);
    const char* wB = (const char*)(wu + cg * 128);
    const int rot = ROTG ? (rt + 2 * cg) & 7 : 0;
#define G_DMAX(j, i)                                                                                      \
    do {                                                                                                  \
        if ((j) < 8) glds16s(xb + (((j) + rot) & 7) * 128 + ((i) >> 1) * (16 * DM * 2), ((i) & 1) ? xoB : xoA,          \
                             lds0 + ((j) & 1) * 16384 + w * 4096 + (i) * 1024);                           \
    } while (0)
#define G_DMAW(u, i)                                                                                      \
    do {                                                                                                  \
        if ((u) < 16) glds16s((((i) >> 1) ? wB : wA) + (unsigned)(((u) + 2 * rot) & 15) * (32 * DF * 2), ((i) & 1) ? wo1 : wo0, \
                              lds0 + G_W + ((u) % 3) * 16384 + w * 4096 + (i) * 1024);                    \
    } while (0)
#pragma unroll
    for (int i = 0; i < 4; ++i) G_DMAX(0, i);
#pragma unroll
    for (int i = 0; i < 4; ++i) G_DMAW(0, i);
    tag = load_epoch(ws) + 1;
    if (job >= 0) kv_job<8>(ws, smem, job, w, lane);
#pragma unroll
    for (int i = 0; i < 4; ++i) G_DMAW(1, i);

    const int ga = 2 * wr + wn, gb = 2 * wr + 1 - wn;
    const int fx = (r >> 1) & 7;
    const int blk = (lane >> 4) & 1, q4 = (lane & 15) >> 2, p4 = lane & 3;
    const int wl0 = G_W + h * 4096 + wn * 1024 + q4 * 128 + (((blk * 2 + (p4 >> 1)) ^ (((q4 >> 1) & 1) << 2)) * 16) + (p4 & 1) * 8;
    const int wl1 = wl0 ^ 64;
    const int c0 = wl0 + wr * 2048, c1 = wl1 + wr * 2048, d0 = wl0 + (1 - wr) * 2048, d1 = wl1 + (1 - wr) * 2048;
    const bf16x8 sel = make_sel(lane);
    f32x16 ac0a = {}, ac0b = {}, ac1a = {}, ac1b = {}, ad0a = {}, ad0b = {}, ad1a = {}, ad1b = {};
    f32x4 cs0 = {}, cs1 = {};
    bf16x8 xa[2], xbf[2], fc0[2], fc1[2], fd0[2], fd1[2];
#define G_XS(u, j) (L + (((u) >> 1) & 1) * 16384 + r * 128 + (((2 * (((u) & 1) * 2 + (j)) + h) ^ fx) * 16))
#define G_WO(u, j) (((u) % 3) * 16384 + (j) * 8192)
#define G_READ(set, u, j)                                                             \
    do {                                                                              \
        fc0[set] = lds_tr2<512>(L + c0 + G_WO(u, j));                                 \
        xa[set] = lds_b128(G_XS(u, j) + ga * 4096);                                   \
        xbf[set] = lds_b128(G_XS(u, j) + gb * 4096);                                  \
        fc1[set] = lds_tr2<512>(L + c1 + G_WO(u, j));                                 \
        fd0[set] = lds_tr2<512>(L + d0 + G_WO(u, j));                                 \
        fd1[set] = lds_tr2<512>(L + d1 + G_WO(u, j));                                 \
        SBAR();                                                                       \
    } while (0)
#define G_STEP(set, RD, nu, nj, D0, D1, D2, D3)                                       \
    do {                                                                              \
        ac0a = MFMA(fc0[set], xa[set], ac0a); SBAR();                                 \
        if (RD) { fc0[1 - set] = lds_tr2<512>(L + c0 + G_WO(nu, nj)); SBAR(); }       \
        ac0b = MFMA(fc0[set], xbf[set], ac0b); SBAR();                                \
        if (RD) { xa[1 - set] = lds_b128(G_XS(nu, nj) + ga * 4096); xbf[1 - set] = lds_b128(G_XS(nu, nj) + gb * 4096); SBAR(); } \
        ac1a = MFMA(fc1[set], xa[set], ac1a); SBAR();                                 \
        if (RD) { fc1[1 - set] = lds_tr2<512>(L + c1 + G_WO(nu, nj)); SBAR(); }       \
        ac1b = MFMA(fc1[set], xbf[set], ac1b); SBAR();                                \
        if (RD) { fd0[1 - set] = lds_tr2<512>(L + d0 + G_WO(nu, nj)); SBAR(); }       \
        ad0a = MFMA(fd0[set], xa[set], ad0a); SBAR();                                 \
        if (RD) { fd1[1 - set] = lds_tr2<512>(L + d1 + G_WO(nu, nj)); SBAR(); }       \
        ad0b = MFMA(fd0[set], xbf[set], ad0b); SBAR();                                \
        D0;                                                                           \
        ad1a = MFMA(fd1[set], xa[set], ad1a); SBAR();                                 \
        D1;                                                                           \
        ad1b = MFMA(fd1[set], xbf[set], ad1b); SBAR();                                \
        D2;                                                                           \
        cs0 = MFMA16(sel, fc0[set], cs0);                                             \
        cs1 = MFMA16(sel, fc1[set], cs1); SBAR();                                     \
        D3;                                                                           \
    } while (0)
    STAMP(1, 1);
    wait_bar<4>();
    STAMP(1, 2);
    if (job >= 0 && threadIdx.x == 0) set_flag(ws, SY_KV + (size_t)job * FL, tag);
    __builtin_amdgcn_s_setprio(G1PRIO);
    if (lane == 0 && w == 1) set_flag(ws, SY_SEEN + (size_t)(256 + bid) * 8, tag);
    G_READ(0, 0, 0);
    G_DMAW(2, 0); G_DMAW(2, 1); G_DMAX(1, 0); G_DMAX(1, 1);
#pragma unroll
    for (int u = 0; u < 16; ++u) {
        if ((u & 1) == 0) G_STEP(0, true, u, 1, G_DMAW(u + 2, 2), G_DMAW(u + 2, 3), G_DMAX(u / 2 + 1, 2), G_DMAX(u / 2 + 1, 3));
        else              G_STEP(0, true, u, 1, G_DMAW(u + 2, 2), G_DMAW(u + 2, 3), (void)0, (void)0);
        if (u < 15) {
            const int nfly = (u + 2 < 16 ? 4 : 0) + ((((u & 1) == 0) && (u / 2 + 1 < 8)) ? 4 : 0);
            if (nfly == 8) wait_bar<8>(); else if (nfly == 4) wait_bar<4>(); else wait_bar<0>();
        }
        if (u & 1) G_STEP(1, u < 15, u + 1, 0, G_DMAW(u + 3, 0), G_DMAW(u + 3, 1), G_DMAX((u + 1) / 2 + 1, 0), G_DMAX((u + 1) / 2 + 1, 1));
        else       G_STEP(1, true, u + 1, 0, G_DMAW(u + 3, 0), G_DMAW(u + 3, 1), (void)0, (void)0);
    }
#undef G_STEP
#undef G_XS
#undef G_WO
#undef G_READ
#undef G_DMAX
#undef G_DMAW
    STAMP(1, 3);
    __builtin_amdgcn_s_setprio(2);
    float* st_cs = (float*)smem;
    if (h == 0) { st_cs[wr * 128 + wn * 64 + r] = cs0[0]; st_cs[wr * 128 + wn * 64 + 32 + r] = cs1[0]; }
    wait_flags(ws, (unsigned)(SY_KV + (rt * 4 + 2 * wr + (lane & 1)) * 2 * FL), tag);
    const __amdgpu_buffer_rsrc_t rst = __builtin_amdgcn_make_buffer_rsrc(ws + WS_STAT + rt * 1024, 0, 1024, 0x00020000);
    const u32x2 sta = __builtin_amdgcn_raw_buffer_load_b64(rst, ga * 256 + r * 8, 0, SC1);
    const u32x2 stb = __builtin_amdgcn_raw_buffer_load_b64(rst, gb * 256 + r * 8, 0, SC1);
    __syncthreads();
    const float mu_a = __uint_as_float(sta[0]), rs_a = __uint_as_float(sta[1]), mu_b = __uint_as_float(stb[0]), rs_b = __uint_as_float(stb[1]);
    {
        const bf16x8 ea = xext(mu_a, h), eb = xext(mu_b, h);
        const bf16x8 ec0 = wext(cs0[0], h), ec1 = wext(cs1[0], h);
        const bf16x8 ed0 = wext(st_cs[(1 - wr) * 128 + wn * 64 + r], h), ed1 = wext(st_cs[(1 - wr) * 128 + wn * 64 + 32 + r], h);
        ac0a = MFMA(ec0, ea, ac0a); ad0a = MFMA(ed0, ea, ad0a);
        ac0b = MFMA(ec0, eb, ac0b); ad0b = MFMA(ed0, eb, ad0b);
        ac1a = MFMA(ec1, ea, ac1a); ad1a = MFMA(ed1, ea, ad1a);
        ac1b = MFMA(ec1, eb, ac1b); ad1b = MFMA(ed1, eb, ad1b);
    }
    const __amdgpu_buffer_rsrc_t rh = __builtin_amdgcn_make_buffer_rsrc(
        ws + WS_ACT + ((size_t)rt * 4 * NKS + 32 + 8 * cg + 4 * wn) * 1024, 0, 4 * NKS * 1024, 0x00020000);
    const float K1c = 2.0f * 0.7978845608028654f * 1.4426950408889634f, K3c = K1c * 0.044715f;
    auto geglu = [&](const f32x16& G, const f32x16& U, float rs, int so) {
        typedef float f32x2 __attribute__((ext_vector_type(2)));
        const float c1_ = -K1c * rs, c3_ = -K3c * rs * rs * rs, ri2 = __builtin_amdgcn_rcpf(rs * rs);
        const f32x2 c1v = {c1_, c1_}, c3v = {c3_, c3_}, riv = {ri2, ri2};
        u32x4 o0, o1;
#pragma unroll
        for (int i = 0; i < 8; ++i) {
            const f32x2 gg = {G[2 * i], G[2 * i + 1]}, uu = {U[2 * i], U[2 * i + 1]};
            const f32x2 t = gg * __builtin_elementwise_fma(gg * gg, c3v, c1v);
            const f32x2 e = {EXP2(t[0]), EXP2(t[1])};
            const f32x2 d = __builtin_elementwise_fma(e, riv, riv);
            const f32x2 rr = {__builtin_amdgcn_rcpf(d[0]), __builtin_amdgcn_rcpf(d[1])};
            const f32x2 hv = (gg * uu) * rr;
            const unsigned pk = cvtpk(hv[0], hv[1]);
            if (i < 4) o0[i] = pk; else o1[i - 4] = pk;
        }
        __builtin_amdgcn_raw_buffer_store_b128(o0, rh, lo16 + so, 0, SC1);
        __builtin_amdgcn_raw_buffer_store_b128(o1, rh, lo16 + so + 1024, 0, SC1);
    };
    const int soa = ga * NKS * 1024, sob = gb * NKS * 1024;
    if (wr == 0) {
        geglu(ac0a, ad0a, rs_a, soa); geglu(ac0b, ad0b, rs_b, sob); geglu(ac1a, ad1a, rs_a, soa + 2048); geglu(ac1b, ad1b, rs_b, sob + 2048);
    } else {
        geglu(ad0a, ac0a, rs_a, soa); geglu(ad0b, ac0b, rs_b, sob); geglu(ad1a, ac1a, rs_a, soa + 2048); geglu(ad1b, ac1b, rs_b, sob + 2048);
    }
    asm volatile("s_waitcnt vmcnt(0) lgkmcnt(0)\n\ts_barrier" ::: "memory");
    if (threadIdx.x == 0) set_flag(ws, SY_H + (size_t)(rt * 16 + cg) * FL, tag);
}

DI void g_retile(const u16* __restrict__ wo, const u16* __restrict__ wd, char* __restrict__ ws, char* smem, const int bid,
                 const int j, const int job, const int w, const int lane, u64& tag) {
    const int r = lane & 31, h = lane >> 5;
    const int ng = j >> 1, ks0 = 72 * (j & 1) + 18 * w;
    const __amdgpu_buffer_rsrc_t rw =
        __builtin_amdgcn_make_buffer_rsrc(ws + WS_W2F + ((size_t)ng * NKS + ks0) * 1024, 0, 18 * 1024, 0x00020000);
    tag = load_epoch(ws) + 1;
    kv_job<0>(ws, smem, job, w, lane);
    if (w == 0) {
        asm volatile("s_waitcnt vmcnt(0)" ::: "memory");
        if (lane == 0) set_flag(ws, SY_KV + (size_t)job * FL, tag);
    }
    __builtin_amdgcn_s_setprio(0);
    if (lane == 0 && w == 1) set_flag(ws, SY_SEEN + (size_t)(256 + bid) * 8, tag);
#pragma unroll 1
    for (int b = 0; b < 3; ++b) {
        u16 v[6][8];
#pragma unroll
        for (int f = 0; f < 6; ++f) {
            const int ks = ks0 + b * 6 + f;
            const u16* src = (ks < 32 ? wo + (size_t)ks * 16 * DM : wd + (size_t)(ks - 32) * 16 * DM) + ng * 32 + r + h * 4 * DM;
#pragma unroll
            for (int e = 0; e < 8; ++e) v[f][e] = src[(8 * (e >> 2) + (e & 3)) * DM];
        }
#pragma unroll
        for (int f = 0; f < 6; ++f) {
            u32x4 o;
#pragma unroll
            for (int i = 0; i < 4; ++i) o[i] = (unsigned)v[f][2 * i] | ((unsigned)v[f][2 * i + 1] << 16);
            __builtin_amdgcn_raw_buffer_store_b128(o, rw, lane * 16 + (b * 6 + f) * 1024, 0, SC1);
        }
    }
    asm volatile("s_waitcnt vmcnt(0)\n\ts_barrier" ::: "memory");
    if (threadIdx.x == 0) set_flag(ws, SY_W + (size_t)j * FL, tag);
    if (j == 0 && w == 0) {
#pragma unroll 1
        for (int i = 0; i < 8; ++i) wait_flags(ws, SY_SEEN + i * 512 + lane * 8, tag);
        if (lane == 0) set_flag(ws, SY_EPOCH, tag);
    }
}

DI void role_g(const u16* __restrict__ x, const u16* __restrict__ wk, const u16* __restrict__ wv, const u16* __restrict__ wo, const u16* __restrict__ wg, const u16* __restrict__ wu,
               const u16* __restrict__ wd, char* __restrict__ ws, u16* __restrict__ out, char* smem, const int bid, const int w,
               const int lane) {
    const int xcd = bid & 7, idx = bid >> 3;
    const int rt = (xcd & 1) * 8 + (idx & 7), cg = (xcd >> 1) * 4 + (idx >> 3);
    const int it = xcd * 4 + (idx & 3), g = idx >> 2;
    const unsigned lo16 = lane * 16;
    u64 tag;
    __builtin_amdgcn_s_setprio(3);
    STAMP(1, 0);
    const int job = idx >= 16 ? xcd * 16 + (idx - 16) : -1;
    if (job >= 0) kv_issue(x, (job & 1) ? wv : wk, smem, job, w, lane);
    if (cg < 14) g_gateup(x, wg, wu, ws, smem, bid, rt, cg, job, w, lane, tag);
    else g_retile(wo, wd, ws, smem, bid, rt * 2 + (cg - 14), job, w, lane, tag);
    STAMP(1, 4);
    __builtin_amdgcn_s_setprio(0);

    wait_flags(ws, lane < 14 ? (unsigned)(SY_H + ((it >> 1) * 16 + lane) * FL) : (unsigned)(SY_W + (4 * g + (lane & 3)) * FL), tag);
    STAMP(1, 5);
    const __amdgpu_buffer_rsrc_t ra =
        __builtin_amdgcn_make_buffer_rsrc(ws + WS_ACT + (size_t)(2 * it) * NKS * 1024, 0, 2 * NKS * 1024, 0x00020000);
    const __amdgpu_buffer_rsrc_t rw =
        __builtin_amdgcn_make_buffer_rsrc(ws + WS_W2F + (size_t)(2 * g) * NKS * 1024, 0, 2 * NKS * 1024, 0x00020000);
    const int so = (32 + 28 * w) * 1024;
    constexpr int PD = 6;
    f32x16 acc00 = {}, acc01 = {}, acc10 = {}, acc11 = {};
    u32x4 fw0[8], fw1[8], fa0[8], fa1[8];
#define G_LOAD(i, k)                                                                                   \
    do {                                                                                               \
        fw0[i] = __builtin_amdgcn_raw_buffer_load_b128(rw, lo16, so + (k) * 1024, SC1);                \
        fa0[i] = __builtin_amdgcn_raw_buffer_load_b128(ra, lo16, so + (k) * 1024, SC1);                \
        fa1[i] = __builtin_amdgcn_raw_buffer_load_b128(ra, lo16, so + (NKS + (k)) * 1024, SC1);        \
        fw1[i] = __builtin_amdgcn_raw_buffer_load_b128(rw, lo16, so + (NKS + (k)) * 1024, SC1);        \
        SBAR();                                                                                        \
    } while (0)
#define G_MMA4(i)                                                                                      \
    do {                                                                                               \
        acc00 = MFMA(BC8(fw0[i]), BC8(fa0[i]), acc00); acc01 = MFMA(BC8(fw0[i]), BC8(fa1[i]), acc01);  \
        acc10 = MFMA(BC8(fw1[i]), BC8(fa0[i]), acc10); acc11 = MFMA(BC8(fw1[i]), BC8(fa1[i]), acc11);  \
        SBAR();                                                                                        \
    } while (0)
#pragma unroll
    for (int i = 0; i < PD; ++i) G_LOAD(i, i);
#pragma unroll
    for (int k = 0; k < 28; ++k) {
        G_MMA4(k % PD);
        if (k + PD < 28) G_LOAD(k % PD, k + PD);
    }
#pragma unroll
    for (int i = 0; i < 8; ++i) {
        fw0[i] = __builtin_amdgcn_raw_buffer_load_b128(rw, lo16, (8 * w + i) * 1024, SC1);
        fw1[i] = __builtin_amdgcn_raw_buffer_load_b128(rw, lo16, (NKS + 8 * w + i) * 1024, SC1);
    }
    u32x4 xv[2];
#pragma unroll
    for (int u = 0; u < 2; ++u) {
        const int t2 = threadIdx.x + 256 * u;
        xv[u] = *(const u32x4*)(x + (size_t)(it * 64 + (t2 >> 3)) * DM + g * 64 + (t2 & 7) * 8);
    }
    STAMP(1, 6);
    wait_flags(ws, (unsigned)(SY_O + (it * 8 + 2 * w + (lane & 1)) * FL), tag);
    STAMP(1, 7);
    __builtin_amdgcn_s_setprio(2);
#pragma unroll
    for (int i = 0; i < 8; ++i) {
        fa0[i] = __builtin_amdgcn_raw_buffer_load_b128(ra, lo16, (8 * w + i) * 1024, SC1);
        fa1[i] = __builtin_amdgcn_raw_buffer_load_b128(ra, lo16, (NKS + 8 * w + i) * 1024, SC1);
    }
    SBAR();
#pragma unroll
    for (int i = 0; i < 8; ++i) G_MMA4(i);
#undef G_LOAD
#undef G_MMA4
    char* dst = smem + w * 16640 + lo16;
#pragma unroll
    for (int q = 0; q < 4; ++q) {
        *(f32x4*)(dst + (0 + q) * 1040) = (f32x4){acc00[4 * q], acc00[4 * q + 1], acc00[4 * q + 2], acc00[4 * q + 3]};
        *(f32x4*)(dst + (4 + q) * 1040) = (f32x4){acc01[4 * q], acc01[4 * q + 1], acc01[4 * q + 2], acc01[4 * q + 3]};
        *(f32x4*)(dst + (8 + q) * 1040) = (f32x4){acc10[4 * q], acc10[4 * q + 1], acc10[4 * q + 2], acc10[4 * q + 3]};
        *(f32x4*)(dst + (12 + q) * 1040) = (f32x4){acc11[4 * q], acc11[4 * q + 1], acc11[4 * q + 2], acc11[4 * q + 3]};
    }
    STAMP(1, 8);
    __syncthreads();
#pragma unroll
    for (int u = 0; u < 2; ++u) {
        const int t2 = threadIdx.x + 256 * u, row = t2 >> 3, c8 = t2 & 7;
        const int a = c8 >> 2, q = c8 & 3, b = row >> 5, rr = row & 31;
        const char* s0 = smem + ((a * 2 + b) * 4 + q) * 1040 + rr * 16;
        f32x4 lo = {__uint_as_float(xv[u][0] << 16), __uint_as_float(xv[u][0] & 0xffff0000u), __uint_as_float(xv[u][1] << 16), __uint_as_float(xv[u][1] & 0xffff0000u)};
        f32x4 hi = {__uint_as_float(xv[u][2] << 16), __uint_as_float(xv[u][2] & 0xffff0000u), __uint_as_float(xv[u][3] << 16), __uint_as_float(xv[u][3] & 0xffff0000u)};
#pragma unroll
        for (int gw = 0; gw < 4; ++gw) {
            lo += *(const f32x4*)(s0 + gw * 16640);
            hi += *(const f32x4*)(s0 + gw * 16640 + 512);
        }
        u32x4 o = {cvtpk(lo[0], lo[1]), cvtpk(lo[2], lo[3]), cvtpk(hi[0], hi[1]), cvtpk(hi[2], hi[3])};
        *(u32x4*)(out + (size_t)(it * 64 + row) * DM + g * 64 + c8 * 8) = o;
    }
    STAMP(1, 9);
}

__global__ __launch_bounds__(256, 2) void fused_block(const u16* __restrict__ x, const u16* __restrict__ wq,
                                                      const u16* __restrict__ wk, const u16* __restrict__ wv,
                                                      const u16* __restrict__ wo, const u16* __restrict__ wg,
                                                      const u16* __restrict__ wu, const u16* __restrict__ wd,
                                                      char* __restrict__ ws, u16* __restrict__ out) {
    __shared__ __attribute__((aligned(16))) char smem[81920];
    const int lane = threadIdx.x & 63;
    const int w = __builtin_amdgcn_readfirstlane(threadIdx.x >> 6);
    if (blockIdx.x < 256) role_a(x, wq, ws, smem, blockIdx.x, w, lane);
    else role_g(x, wk, wv, wo, wg, wu, wd, ws, out, smem, blockIdx.x - 256, w, lane);
}

extern "C" void kernel_launch(void* const* d_in, const int* in_sizes, int n_in, void* d_out, int out_size, void* d_ws,
                              size_t ws_size, hipStream_t stream) {
    fused_block<<<512, 256, 0, stream>>>((const u16*)d_in[0], (const u16*)d_in[1], (const u16*)d_in[2], (const u16*)d_in[3],
                                         (const u16*)d_in[4], (const u16*)d_in[6], (const u16*)d_in[7], (const u16*)d_in[8],
                                         (char*)d_ws, (u16*)d_out);
}
```

```cpp
#include <hip/hip_runtime.h>
#include <hip/hip_bf16.h>
#include <stdint.h>

typedef __bf16 bf16x8 __attribute__((ext_vector_type(8)));
typedef __bf16 bf16x2 __attribute__((ext_vector_type(2)));
typedef short s16x4 __attribute__((ext_vector_type(4)));
typedef float f32x16 __attribute__((ext_vector_type(16)));
typedef float f32x4 __attribute__((ext_vector_type(4)));
typedef unsigned u32x4 __attribute__((ext_vector_type(4)));
typedef unsigned u32x2 __attribute__((ext_vector_type(2)));
typedef unsigned short u16;
typedef unsigned long long u64;
typedef __attribute__((address_space(1))) u64 gu64;
#define LAS __attribute__((address_space(3)))
#define DI __device__ __forceinline__
#define MFMA(a, b, c) __builtin_amdgcn_mfma_f32_32x32x16_bf16((a), (b), (c), 0, 0, 0)
#define MFMA16(a, b, c) __builtin_amdgcn_mfma_f32_16x16x32_bf16((a), (b), (c), 0, 0, 0)
#define SBAR() __builtin_amdgcn_sched_barrier(0)
#define EXP2(v) __builtin_amdgcn_exp2f(v)
#define BC8(v) __builtin_bit_cast(bf16x8, v)
#define SC1 16

constexpr int DM = 512, DF = 1792;
constexpr int NKS = 144;
constexpr size_t WS_KF = 0;
constexpr size_t WS_VF = 65536;
constexpr size_t WS_STAT = 131072;
constexpr size_t WS_CS = 131072 + 16384;
constexpr size_t WS_SYNC = 131072 + 32768;
constexpr size_t SY_EPOCH = 0, SY_SEEN = 4096, SY_W = 8192, SY_C = 12288, SY_KV = 16384, SY_O = 32768, SY_H = 65536;
constexpr int FL = 128;
constexpr size_t WS_ACT = 262144;
constexpr size_t WS_W2F = WS_ACT + (size_t)64 * NKS * 1024;
constexpr size_t WS_END = WS_W2F + (size_t)16 * NKS * 1024;
constexpr unsigned SPIN_CAP = 1u << 18;
#define G2PRIO 0
#define G2PD 6
#define PSLEEP 2
#define NLOWJ 2
#define NLOWN 3
#define G1PRIO 0
#define ROTG 0

#define STAMP(kern, i)

DI unsigned cvtpk(float lo, float hi) {
    bf16x2 r = {(__bf16)lo, (__bf16)hi};
    return __builtin_bit_cast(unsigned, r);
}
DI float bf_round(float v) { return (float)(__bf16)v; }
DI float swap_add(float v) {
    auto rr = __builtin_amdgcn_permlane32_swap(__float_as_uint(v), __float_as_uint(v), false, false);
    return __uint_as_float(rr[0]) + __uint_as_float(rr[1]);
}
DI float swap_max(float v) {
    auto rr = __builtin_amdgcn_permlane32_swap(__float_as_uint(v), __float_as_uint(v), false, false);
    return __builtin_fmaxf(__uint_as_float(rr[0]), __uint_as_float(rr[1]));
}
DI void glds16s(const void* sbase, unsigned voff, unsigned lds_base) {
    asm volatile("s_mov_b32 m0, %2\n\ts_nop 0\n\tglobal_load_lds_dwordx4 %0, %1" ::"v"(voff), "s"(sbase), "s"(lds_base) : "memory");
    SBAR();
}
template <int N> DI void wait_bar() { asm volatile("s_waitcnt vmcnt(%0) lgkmcnt(0)\n\ts_barrier" ::"n"(N) : "memory"); }
DI bf16x8 lds_b128(LAS const char* p) { return *(LAS const bf16x8*)p; }
template <int STEP> DI bf16x8 lds_tr2(LAS const char* p) {
    s16x4 lo = __builtin_amdgcn_ds_read_tr16_b64_v4i16((LAS s16x4*)p);
    s16x4 hi = __builtin_amdgcn_ds_read_tr16_b64_v4i16((LAS s16x4*)(p + STEP));
    return __builtin_bit_cast(bf16x8, __builtin_shufflevector(lo, hi, 0, 1, 2, 3, 4, 5, 6, 7));
}
DI u32x4 pack8(const f32x16& v, int o, float s) {
    u32x4 r;
#pragma unroll
    for (int i = 0; i < 4; ++i) r[i] = cvtpk(v[o + 2 * i] * s, v[o + 2 * i + 1] * s);
    return r;
}
DI void ln_stat(float sum, float sq, float& mu, float& rs) {
    const float mean = sum * (1.0f / DM);
    const float var = __builtin_fmaxf(sq * (1.0f / DM) - mean * mean, 0.f);
    const float sd = bf_round(__builtin_amdgcn_sqrtf(bf_round(bf_round(var) + 1e-5f)));
    mu = bf_round(mean); rs = __builtin_amdgcn_rcpf(sd);
}
DI bf16x8 wext(float cs, int h) {
    const float hi = bf_round(cs);
    u32x4 v = {h == 0 ? cvtpk(hi, cs - hi) : 0u, 0u, 0u, 0u};
    return BC8(v);
}
DI bf16x8 xext(float mu, int h) {
    u32x4 v = {h == 0 ? cvtpk(-mu, -mu) : 0u, 0u, 0u, 0u};
    return BC8(v);
}
DI bf16x8 make_sel(int lane) {
    const unsigned o1 = ((lane & 3) == 0 && ((lane >> 2) & 1) == ((lane >> 4) & 1)) ? 0x3F803F80u : 0u;
    return BC8(((u32x4){o1, o1, o1, o1}));
}
DI float diag32(const f32x16& a, int r) {
    typedef float f32x8 __attribute__((ext_vector_type(8)));
    typedef float f32x2 __attribute__((ext_vector_type(2)));
    const f32x8 t8 = (r & 16) ? __builtin_shufflevector(a, a, 8, 9, 10, 11, 12, 13, 14, 15)
                              : __builtin_shufflevector(a, a, 0, 1, 2, 3, 4, 5, 6, 7);
    const f32x4 t4 = (r & 8) ? __builtin_shufflevector(t8, t8, 4, 5, 6, 7) : __builtin_shufflevector(t8, t8, 0, 1, 2, 3);
    const f32x2 t2 = (r & 2) ? __builtin_shufflevector(t4, t4, 2, 3) : __builtin_shufflevector(t4, t4, 0, 1);
    const float t20 = t2[0], t21 = t2[1];
    return (r & 1) ? t21 : t20;
}
DI u64 load_epoch(const char* ws) {
    u64 e;
    asm volatile("s_load_dwordx2 %0, %1, 0x0\n\ts_waitcnt lgkmcnt(0)" : "=s"(e) : "s"(ws + WS_SYNC + SY_EPOCH) : "memory");
    return e;
}
DI void set_flag(char* ws, size_t off, u64 tag) {
    __hip_atomic_store((gu64*)(ws + WS_SYNC + off), tag, __ATOMIC_RELAXED, __HIP_MEMORY_SCOPE_AGENT);
}
DI void wait_flags(char* ws, unsigned off, u64 tag) {
    gu64* p = (gu64*)(ws + WS_SYNC + off);
    for (unsigned spins = 0; spins < SPIN_CAP; ++spins) {
        const u64 v = __hip_atomic_load(p, __ATOMIC_RELAXED, __HIP_MEMORY_SCOPE_AGENT);
        if (__all(v == tag)) break;
        __builtin_amdgcn_s_sleep(PSLEEP);
    }
    __builtin_amdgcn_fence(__ATOMIC_ACQUIRE, "wavefront");
    asm volatile("" ::: "memory");
}
DI float max16(const f32x16& s) {
    float a = __builtin_fmaxf(s[0], s[1]);
#pragma unroll
    for (int i = 2; i < 16; i += 2) a = __builtin_fmaxf(__builtin_fmaxf(a, s[i]), s[i + 1]);
    return a;
}

DI f32x16 attn_slow(const __amdgpu_buffer_rsrc_t rkv, const bf16x8 q, const int lane) {
    const int r = lane & 31, h = lane >> 5;
    const unsigned vo = (unsigned)WS_VF + h * 256 + (r & 15) * 16;
    float m = -3.0e38f;
    f32x16 o = {};
#pragma unroll 1
    for (int t = 0; t < 64; ++t) {
        const u32x4 kf = __builtin_amdgcn_raw_buffer_load_b128(rkv, lane * 16, t * 1024, SC1);
        u32x4 v0 = __builtin_amdgcn_raw_buffer_load_b128(rkv, vo, t * 1024, SC1);
        u32x4 v1 = __builtin_amdgcn_raw_buffer_load_b128(rkv, vo + 512, t * 1024, SC1);
        if (r >= 16) { const unsigned c_ = r == 16 ? 0x3F803F80u : 0u; v0 = (u32x4){c_, c_, c_, c_}; v1 = v0; }
        f32x16 z = {};
        f32x16 s = MFMA(BC8(kf), q, z);
        const float mn = __builtin_fmaxf(m, swap_max(max16(s)));
        const float al = EXP2(m - mn);
        m = mn;
#pragma unroll
        for (int i = 0; i < 16; ++i) { s[i] -= mn; o[i] *= al; }
        u32x4 p0, p1;
#pragma unroll
        for (int i = 0; i < 4; ++i) {
            p0[i] = cvtpk(EXP2(s[2 * i]), EXP2(s[2 * i + 1]));
            p1[i] = cvtpk(EXP2(s[8 + 2 * i]), EXP2(s[8 + 2 * i + 1]));
        }
        o = MFMA(BC8(v0), BC8(p0), o);
        o = MFMA(BC8(v1), BC8(p1), o);
    }
    return o;
}

constexpr int A_STAGE = 16384;
constexpr int VT = 1152, VH = 288;
constexpr int A_SLOT = 8 * 1024 + 8 * VT;
DI void role_a(const u16* __restrict__ x, const u16* __restrict__ wq, char* __restrict__ ws, char* smem, const int bid, const int w, const int lane) {
    const int xcd = bid & 7, idx = bid >> 3;
    const int it = xcd * 4 + (idx & 3), g = idx >> 2;
    const int p = w >> 1, rgl = w & 1;
    const int r = lane & 31, h = lane >> 5;
    const unsigned lds0 = (unsigned)(uintptr_t)smem;
    const unsigned lo16 = lane * 16;
    LAS const char* L = (LAS const char*)smem;
    __builtin_amdgcn_s_setprio(2);
    STAMP(0, 0);

    const int prow = lane >> 3, pslot = lane & 7;
    const unsigned xo0 = (unsigned)((16 * w + prow) * DM * 2 + ((pslot ^ (((16 * w + prow) >> 1) & 7)) * 16));
    const unsigned xo1 = (unsigned)((16 * w + 8 + prow) * DM * 2 + ((pslot ^ (((16 * w + 8 + prow) >> 1) & 7)) * 16));
    const unsigned wo0 = (unsigned)((16 * w + prow) * DM * 2 + ((pslot ^ (((prow >> 1) & 1) << 2)) * 16));
    const unsigned wo1 = wo0 + 8 * DM * 2;
    const char* xb = (const char*)(x + (size_t)it * 64 * DM);
    const char* wb = (const char*)(wq + g * 64);
    const int blk = (lane >> 4) & 1, q4 = (lane & 15) >> 2, p4 = lane & 3;
    const bf16x8 sel = make_sel(lane);
#define A_DMA(s, i)                                                                                       \
    do {                                                                                                  \
        if ((s) < 8) {                                                                                    \
            const unsigned sb_ = lds0 + ((s) & 3) * A_STAGE + w * 2048;                                   \
            if ((i) == 0) glds16s(xb + (s) * 128, xo0, sb_);                                              \
            if ((i) == 1) glds16s(xb + (s) * 128, xo1, sb_ + 1024);                                       \
            if ((i) == 2) glds16s(wb + (s) * (64 * DM * 2), wo0, sb_ + 8192);                             \
            if ((i) == 3) glds16s(wb + (s) * (64 * DM * 2), wo1, sb_ + 8192 + 1024);                      \
        }                                                                                                 \
    } while (0)
#define A_DMA4(s) do { A_DMA(s, 0); A_DMA(s, 1); A_DMA(s, 2); A_DMA(s, 3); } while (0)
    A_DMA4(0); A_DMA4(1); A_DMA4(2); A_DMA4(3);
    const u64 tag = load_epoch(ws) + 1;

    const int fx = (r >> 1) & 7;
    const int wlane = 8192 + h * 1024 + q4 * 128 + (((p * 4 + blk * 2 + (p4 >> 1)) ^ (((q4 >> 1) & 1) << 2)) * 16) + (p4 & 1) * 8;
    f32x16 accq = {};
    f32x4 acc_cs = {};
    bf16x8 xa[2], wc[2];
#define A_READ(set, s, j)                                                             \
    do {                                                                              \
        LAS const char* sb_ = L + ((s) & 3) * A_STAGE;                                \
        wc[set] = lds_tr2<512>(sb_ + wlane + (j) * 2048);                             \
        xa[set] = lds_b128(sb_ + (rgl * 32 + r) * 128 + (((2 * (j) + h) ^ fx) * 16)); \
        SBAR();                                                                       \
    } while (0)
#define A_MMA(set, j, ds, d0)                                                         \
    do {                                                                              \
        accq = MFMA(wc[set], xa[set], accq);                                          \
        acc_cs = MFMA16(sel, wc[set], acc_cs);                                        \
        SBAR();                                                                       \
        A_DMA(ds, d0);                                                                \
    } while (0)
    STAMP(0, 1);
    wait_bar<12>();
    STAMP(0, 2);
    if (lane == 0 && w == 1) set_flag(ws, SY_SEEN + (size_t)bid * 8, tag);
    A_READ(0, 0, 0);
#pragma unroll
    for (int s = 0; s < 8; ++s) {
        A_READ(1, s, 1); A_MMA(0, 0, (s >= 1 ? s + 3 : 8), 1);
        A_READ(0, s, 2); A_MMA(1, 1, (s >= 1 ? s + 3 : 8), 2);
        A_READ(1, s, 3); A_MMA(0, 2, (s >= 1 ? s + 3 : 8), 3);
        if (s < 7) {
            if (s <= 4) wait_bar<8>(); else if (s == 5) wait_bar<4>(); else wait_bar<0>();
            A_READ(0, s + 1, 0);
        }
        A_MMA(1, 3, s + 4, 0);
    }
#undef A_READ
#undef A_MMA
#undef A_DMA
#undef A_DMA4
    STAMP(0, 3);

    const __amdgpu_buffer_rsrc_t rkv = __builtin_amdgcn_make_buffer_rsrc(ws, 0, 131072, 0x00020000);
    gu64* kvf = (gu64*)(ws + WS_SYNC + SY_KV +
                        (size_t)(lane < 32 ? ((lane >> 2) * 8 + 2 * w) * 2 + (lane & 3) : (2 * it + rgl) * 2) * FL);
    unsigned long long rdy = 0;
    for (unsigned spins = 0; spins < SPIN_CAP; ++spins) {
        rdy = __ballot(__hip_atomic_load(kvf, __ATOMIC_RELAXED, __HIP_MEMORY_SCOPE_AGENT) == tag);
        if ((rdy & 0x100000FFFull) == 0x100000FFFull) break;
        __builtin_amdgcn_s_sleep(1);
    }
    __builtin_amdgcn_fence(__ATOMIC_ACQUIRE, "wavefront");
    asm volatile("" ::: "memory");
    STAMP(0, 5);
    bf16x8 qa, qb;
    const __amdgpu_buffer_rsrc_t rst = __builtin_amdgcn_make_buffer_rsrc(ws + WS_STAT + (2 * it + rgl) * 256, 0, 256, 0x00020000);
    const u32x2 stq = __builtin_amdgcn_raw_buffer_load_b64(rst, r * 8, 0, SC1);
    u32x4 sk0, sk1, sv0, sv1;
#define KV_LOAD(c)                                                                                     \
    do {                                                                                               \
        const int so_ = ((c) * 8 + 2 * w) * 1024;                                                      \
        sk0 = __builtin_amdgcn_raw_buffer_load_b128(rkv, lo16, so_, SC1);                              \
        sk1 = __builtin_amdgcn_raw_buffer_load_b128(rkv, lo16 + 1024, so_, SC1);                       \
        sv0 = __builtin_amdgcn_raw_buffer_load_b128(rkv, lo16 + (unsigned)WS_VF, so_, SC1);            \
        sv1 = __builtin_amdgcn_raw_buffer_load_b128(rkv, lo16 + (unsigned)WS_VF + 1024, so_, SC1);     \
    } while (0)
    const int kwr = w * 2048 + lo16, vwr = 8192 + w * 2 * VT + (lane >> 4) * VH + (lane & 15) * 16;
#define KV_WRITE(slotbase)                                                                             \
    do {                                                                                               \
        *(LAS u32x4*)(slotbase + kwr) = sk0; *(LAS u32x4*)(slotbase + kwr + 1024) = sk1;               \
        *(LAS u32x4*)(slotbase + vwr) = sv0; *(LAS u32x4*)(slotbase + vwr + VT) = sv1;                 \
    } while (0)
    LAS char* LW = (LAS char*)smem;
#define KV_CONST(s3)                                                                                   \
    do {                                                                                               \
        const unsigned c_ = (lane & 32) ? 0u : 0x3F803F80u;                                            \
        *(LAS u32x4*)(LW + (s3) * A_SLOT + 8192 + (lane & 31) * VH + 256 + ((lane & 32) >> 1)) = (u32x4){c_, c_, c_, c_}; \
    } while (0)
    {
        u32x4 tk0, tk1, tv0, tv1, uk0, uk1, uv0, uv1;
        KV_LOAD(0);
        tk0 = sk0; tk1 = sk1; tv0 = sv0; tv1 = sv1;
        KV_LOAD(1);
        uk0 = sk0; uk1 = sk1; uv0 = sv0; uv1 = sv1;
        KV_LOAD(2);
        SBAR();
        if (threadIdx.x < 128) KV_CONST(threadIdx.x >> 6);
        accq = MFMA(wext(acc_cs[0], h), xext(__uint_as_float(stq[0]), h), accq);
        const float qs = __uint_as_float(stq[1]) * (0.25f * 1.4426950408889634f);
        qa = BC8(pack8(accq, 0, qs));
        qb = BC8(pack8(accq, 8, qs));
        SBAR();
        *(LAS u32x4*)(LW + kwr) = tk0; *(LAS u32x4*)(LW + kwr + 1024) = tk1;
        *(LAS u32x4*)(LW + vwr) = tv0; *(LAS u32x4*)(LW + vwr + VT) = tv1;
        *(LAS u32x4*)(LW + A_SLOT + kwr) = uk0; *(LAS u32x4*)(LW + A_SLOT + kwr + 1024) = uk1;
        *(LAS u32x4*)(LW + A_SLOT + vwr) = uv0; *(LAS u32x4*)(LW + A_SLOT + vwr + VT) = uv1;
    }
    asm volatile("s_waitcnt lgkmcnt(0)\n\ts_barrier" ::: "memory");
    STAMP(0, 6);
    if (w == 2) KV_CONST(2);
    const int kln = lo16, vln = 8192 + h * VH + (r < 17 ? r : 17) * 16;
    f32x16 nma, nmb;
    bf16x8 kf = lds_b128(L + kln);
    bf16x8 v0 = lds_b128(L + vln), v1 = lds_b128(L + vln + 2 * VH);
    {
        f32x16 z = {};
        f32x16 s0 = MFMA(kf, qa, z), s1 = MFMA(kf, qb, z);
        const float ma = swap_max(max16(s0)), mb = swap_max(max16(s1));
#pragma unroll
        for (int i = 0; i < 16; ++i) { nma[i] = -ma; nmb[i] = -mb; }
    }
    f32x16 oa = {}, ob = {};
    f32x16 sa = MFMA(kf, qa, nma), sb;
    float e0, e1;
    u32x4 pp = {0u, 0u, 0u, 0u};
    bf16x8 vq = v1;
    e0 = EXP2(sa[0]); e1 = EXP2(sa[1]);
#define ATT_HALF(SX, OX, OP, SY, NEXT, V0, V1, LOADS)                                                   \
    do {                                                                                                \
        OP = MFMA(vq, BC8(pp), OP); SBAR();                                                             \
        float e2 = EXP2(SX[2]), e3 = EXP2(SX[3]); LOADS; float e4 = EXP2(SX[4]), e5 = EXP2(SX[5]); SBAR(); \
        SY = NEXT; SBAR();                                                                              \
        float e6 = EXP2(SX[6]), e7 = EXP2(SX[7]);                                                       \
        u32x4 p0; p0[0] = cvtpk(e0, e1); p0[1] = cvtpk(e2, e3); p0[2] = cvtpk(e4, e5); p0[3] = cvtpk(e6, e7); SBAR(); \
        e0 = EXP2(SX[8]); e1 = EXP2(SX[9]); SBAR();                                                     \
        OX = MFMA(V0, BC8(p0), OX); SBAR();                                                             \
        e2 = EXP2(SX[10]); e3 = EXP2(SX[11]); e4 = EXP2(SX[12]); e5 = EXP2(SX[13]); e6 = EXP2(SX[14]); e7 = EXP2(SX[15]); \
        pp[0] = cvtpk(e0, e1); pp[1] = cvtpk(e2, e3); pp[2] = cvtpk(e4, e5); pp[3] = cvtpk(e6, e7); vq = V1; SBAR(); \
        e0 = EXP2(SY[0]); e1 = EXP2(SY[1]); SBAR();                                                     \
    } while (0)
#define ATT_TILE(KN, VN, EXTRA)                                                                         \
    do {                                                                                                \
        bf16x8 kn, n0, n1;                                                                              \
        ATT_HALF(sa, oa, ob, sb, MFMA(kf, qb, nmb), v0, v1, kn = lds_b128(KN); EXTRA);                  \
        ATT_HALF(sb, ob, oa, sa, MFMA(kn, qa, nma), v0, v1, (n0 = lds_b128(VN), n1 = lds_b128((VN) + 2 * VH))); \
        kf = kn; v0 = n0; v1 = n1;                                                                      \
    } while (0)
    __builtin_amdgcn_s_setprio(1);
    int cur = 0, nxt = A_SLOT, fre = 2 * A_SLOT;
#pragma unroll 1
    for (int c = 0; c < 8; ++c) {
        LAS const char* kc = L + cur + kln;
        LAS const char* vc = L + cur + vln;
        u64 fv = tag;
        if (rdy != ~0ull) fv = __hip_atomic_load(kvf, __ATOMIC_RELAXED, __HIP_MEMORY_SCOPE_AGENT);
        SBAR();
        ATT_TILE(kc + 1 * 1024, vc + 1 * VT, (void)0);
        ATT_TILE(kc + 2 * 1024, vc + 2 * VT, KV_WRITE((LW + fre)));
        if (rdy != ~0ull) {
            rdy = __ballot(fv == tag);
            const unsigned long long need = c < 5 ? 0xFull << (4 * (c + 3)) : 0ull;
            for (unsigned spins = 0; (rdy & need) != need && spins < SPIN_CAP; ++spins)
                rdy = __ballot(__hip_atomic_load(kvf, __ATOMIC_RELAXED, __HIP_MEMORY_SCOPE_AGENT) == tag);
            __builtin_amdgcn_fence(__ATOMIC_ACQUIRE, "wavefront");
            asm volatile("" ::: "memory");
        }
        SBAR();
        ATT_TILE(kc + 3 * 1024, vc + 3 * VT, KV_LOAD(c + 3));
        ATT_TILE(kc + 4 * 1024, vc + 4 * VT, (void)0);
        ATT_TILE(kc + 5 * 1024, vc + 5 * VT, (void)0);
        ATT_TILE(kc + 6 * 1024, vc + 6 * VT, (void)0);
        ATT_TILE(kc + 7 * 1024, vc + 7 * VT, (void)0);
        ATT_TILE(L + nxt + kln, L + nxt + vln, (void)0);
        asm volatile("s_waitcnt lgkmcnt(0)\n\ts_barrier" ::: "memory");
        const int t_ = cur; cur = nxt; nxt = fre; fre = t_;
    }
    ob = MFMA(vq, BC8(pp), ob);
    STAMP(0, 7);
#undef ATT_TILE
#undef ATT_HALF
#undef KV_LOAD
#undef KV_WRITE
#undef KV_CONST
    float la, lb;
    {
        la = swap_add(oa[8]); lb = swap_add(ob[8]);
        const bool bad = !(la > 9.094947e-13f && la < 1.0995116e12f && lb > 9.094947e-13f && lb < 1.0995116e12f);
        if (__builtin_expect(__any(bad), 0)) {
            oa = attn_slow(rkv, qa, lane); ob = attn_slow(rkv, qb, lane);
            la = swap_add(oa[8]); lb = swap_add(ob[8]);
        }
    }
    const __amdgpu_buffer_rsrc_t ro = __builtin_amdgcn_make_buffer_rsrc(
        ws + WS_ACT + ((size_t)(2 * it + rgl) * NKS + 4 * g + 2 * p) * 1024, 0, 2048, 0x00020000);
    __builtin_amdgcn_raw_buffer_store_b128(pack8(oa, 0, __builtin_amdgcn_rcpf(la)), ro, lo16, 0, SC1);
    __builtin_amdgcn_raw_buffer_store_b128(pack8(ob, 0, __builtin_amdgcn_rcpf(lb)), ro, lo16 + 1024, 0, SC1);
    asm volatile("s_waitcnt vmcnt(0)\n\ts_barrier" ::: "memory");
    if (threadIdx.x == 0) set_flag(ws, SY_O + (size_t)(it * 8 + g) * FL, tag);
    STAMP(0, 8);
}

constexpr int J_X = 49152, J_W = 16384;
DI void kv_issue(const u16* __restrict__ x, const u16* __restrict__ wkv, char* smem, const int job, const int w, const int lane) {
    const unsigned lds0 = (unsigned)(uintptr_t)smem;
    const int prow = lane >> 3, pslot = lane & 7;
    const unsigned joA = (unsigned)(prow * DM * 2 + ((pslot ^ (prow >> 1)) * 16));
    const unsigned joB = (unsigned)((8 + prow) * DM * 2 + ((pslot ^ (4 + (prow >> 1))) * 16));
    const char* xj = (const char*)(x + (size_t)(job >> 1) * 32 * DM) + w * 256;
    const char* kvw = (const char*)wkv + w * 4096;
#pragma unroll
    for (int sl = 0; sl < 2; ++sl) {
#pragma unroll
        for (int i = 0; i < 4; ++i)
            glds16s(xj + sl * 128 + (i >> 1) * (16 * DM * 2), (i & 1) ? joB : joA, lds0 + J_X + w * 8192 + sl * 4096 + i * 1024);
        glds16s(kvw + sl * 2048, lane * 16, lds0 + J_W + w * 4096 + sl * 2048);
        glds16s(kvw + sl * 2048 + 1024, lane * 16, lds0 + J_W + w * 4096 + sl * 2048 + 1024);
    }
}
template <int YOUNG>
DI void kv_job(char* __restrict__ ws, char* smem, const int job, const int w, const int lane) {
    const int r = lane & 31, h = lane >> 5, q4 = (lane & 15) >> 2, p4 = lane & 3;
    const int rg = job >> 1;
    const bool isv = job & 1;
    const unsigned lo16 = lane * 16;
    LAS const char* L = (LAS const char*)smem;
    const bf16x8 sel = make_sel(lane);
    LAS const char* xl = L + J_X + w * 8192 + r * 128;
    LAS const char* kl = L + J_W + w * 4096 + (8 * h + q4) * 32 + p4 * 8;
    const int fj = (r >> 1) & 7;
    f32x16 acck = {}, accx = {};
    f32x4 acc_ck = {}, acc_rs = {};
    bf16x8 xf[8], kf[8];
    asm volatile("s_waitcnt vmcnt(%0)" ::"n"(6 + YOUNG) : "memory");
#pragma unroll
    for (int k = 0; k < 4; ++k) { xf[k] = lds_b128(xl + (((2 * k + h) ^ fj) * 16)); kf[k] = lds_tr2<128>(kl + k * 512); }
    asm volatile("s_waitcnt vmcnt(%0)" ::"n"(YOUNG) : "memory");
#pragma unroll
    for (int k = 4; k < 8; ++k) { xf[k] = lds_b128(xl + 4096 + (((2 * (k - 4) + h) ^ fj) * 16)); kf[k] = lds_tr2<128>(kl + k * 512); }
    if (isv) {
#pragma unroll
        for (int k = 0; k < 8; ++k) acck = MFMA(xf[k], kf[k], acck);
    } else {
#pragma unroll
        for (int k = 0; k < 8; ++k) acck = MFMA(kf[k], xf[k], acck);
    }
#pragma unroll
    for (int k = 0; k < 8; ++k) {
        accx = MFMA(xf[k], xf[k], accx);
        acc_rs = MFMA16(sel, xf[k], acc_rs);
        acc_ck = MFMA16(sel, kf[k], acc_ck);
    }
    char* pq = smem + J_W + w * 4096;
    if (isv) {
        if (r < 16) {
#pragma unroll
            for (int q = 0; q < 4; ++q)
                *(f32x4*)(pq + q * 512 + (h * 16 + r) * 16) = (f32x4){acck[4 * q], acck[4 * q + 1], acck[4 * q + 2], acck[4 * q + 3]};
        }
    } else {
        *(f32x4*)(pq + lo16) = (f32x4){acck[0], acck[1], acck[2], acck[3]};
        *(f32x4*)(pq + 1024 + lo16) = (f32x4){acck[4], acck[5], acck[6], acck[7]};
    }
    const float dg = diag32(accx, r);
    if (h == ((r >> 2) & 1)) { ((float*)(pq + 2048))[r] = acc_rs[0]; ((float*)(pq + 2176))[r] = dg; }
    if (lane < 16) ((float*)(pq + 2304))[lane] = acc_ck[0];
    asm volatile("s_waitcnt lgkmcnt(0)\n\ts_barrier" ::: "memory");
    if (w == 0) {
        const char* p0 = smem + J_W;
        float sum = 0.f, sq = 0.f, ck = 0.f;
#pragma unroll
        for (int v = 0; v < 4; ++v) {
            sum += ((const float*)(p0 + v * 4096 + 2048))[r];
            sq += ((const float*)(p0 + v * 4096 + 2176))[r];
            ck += ((const float*)(p0 + v * 4096 + 2304))[lane & 15];
        }
        float mu, rs;
        ln_stat(sum, sq, mu, rs);
        const bf16x8 ex = xext(mu, h), ek = wext(ck, h);
        const __amdgpu_buffer_rsrc_t rkv_st = __builtin_amdgcn_make_buffer_rsrc(ws, 0, 131072 + 16384, 0x00020000);
        if (isv) {
            float* srs = (float*)(smem + J_W + 2432);
            if (h == 0) srs[r] = rs;
#pragma unroll
            for (int v = 1; v < 4; ++v)
#pragma unroll
                for (int q = 0; q < 4; ++q) {
                    const f32x4 o = *(const f32x4*)(p0 + v * 4096 + q * 512 + (h * 16 + (r & 15)) * 16);
                    acck[4 * q] += o[0]; acck[4 * q + 1] += o[1]; acck[4 * q + 2] += o[2]; acck[4 * q + 3] += o[3];
                }
            acck = MFMA(ex, ek, acck);
            f32x16 v;
#pragma unroll
            for (int q = 0; q < 4; ++q) {
                const f32x4 rr = *(const f32x4*)(srs + 8 * q + 4 * h);
#pragma unroll
                for (int i = 0; i < 4; ++i) v[4 * q + i] = acck[4 * q + i] * rr[i];
            }
            const unsigned vo = r < 16 ? (unsigned)WS_VF + h * 256 + r * 16 : 0x40000000u;
            __builtin_amdgcn_raw_buffer_store_b128(pack8(v, 0, 1.0f), rkv_st, vo + rg * 1024, 0, SC1);
            __builtin_amdgcn_raw_buffer_store_b128(pack8(v, 8, 1.0f), rkv_st, vo + rg * 1024 + 512, 0, SC1);
        } else {
#pragma unroll
            for (int v = 1; v < 4; ++v)
#pragma unroll
                for (int q = 0; q < 2; ++q) {
                    const f32x4 o = *(const f32x4*)(p0 + v * 4096 + q * 1024 + lo16);
                    acck[4 * q] += o[0]; acck[4 * q + 1] += o[1]; acck[4 * q + 2] += o[2]; acck[4 * q + 3] += o[3];
                }
            acck = MFMA(ek, ex, acck);
            u32x2 ms = {__float_as_uint(mu), __float_as_uint(rs)};
            __builtin_amdgcn_raw_buffer_store_b64(ms, rkv_st, h == 0 ? (unsigned)WS_STAT + rg * 256 + r * 8 : 0x40000000u, 0, SC1);
            __builtin_amdgcn_raw_buffer_store_b128(pack8(acck, 0, rs), rkv_st, lo16 + rg * 1024, 0, SC1);
        }
    }
}

constexpr int G_W = 32768;
DI void g_gateup(const u16* __restrict__ x, const u16* __restrict__ wg, const u16* __restrict__ wu, char* __restrict__ ws,
                 char* smem, const int bid, const int rt, const int cg, const int job, const int w, const int lane, u64& tag) {
    const int wr = w & 1, wn = w >> 1;
    const int r = lane & 31, h = lane >> 5;
    const unsigned lds0 = (unsigned)(uintptr_t)smem;
    const unsigned lo16 = lane * 16;
    LAS const char* L = (LAS const char*)smem;
    const int prow = lane >> 3, pslot = lane & 7;
    const unsigned xoA = (unsigned)((32 * w + prow) * DM * 2 + ((pslot ^ (prow >> 1)) * 16));
    const unsigned xoB = (unsigned)((32 * w + 8 + prow) * DM * 2 + ((pslot ^ (4 + (prow >> 1))) * 16));
    const unsigned wo0 = (unsigned)((8 * w + prow) * DF * 2 + ((pslot ^ (((prow >> 1) & 1) << 2)) * 16));
    const unsigned wo1 = wo0 + 128;
    const char* xb = (const char*)(x + (size_t)rt * 128 * DM);
    const char* wA = (const char*)(wg + cg * 128);
    const char* wB = (const char*)(wu + cg * 128);
    const int rot = ROTG ? (rt + 2 * cg) & 7 : 0;
#define G_DMAX(j, i)                                                                                      \
    do {                                                                                                  \
        if ((j) < 8) glds16s(xb + (((j) + rot) & 7) * 128 + ((i) >> 1) * (16 * DM * 2), ((i) & 1) ? xoB : xoA,          \
                             lds0 + ((j) & 1) * 16384 + w * 4096 + (i) * 1024);                           \
    } while (0)
#define G_DMAW(u, i)                                                                                      \
    do {                                                                                                  \
        if ((u) < 16) glds16s((((i) >> 1) ? wB : wA) + (unsigned)(((u) + 2 * rot) & 15) * (32 * DF * 2), ((i) & 1) ? wo1 : wo0, \
                              lds0 + G_W + ((u) % 3) * 16384 + w * 4096 + (i) * 1024);                    \
    } while (0)
#pragma unroll
    for (int i = 0; i < 4; ++i) G_DMAX(0, i);
#pragma unroll
    for (int i = 0; i < 4; ++i) G_DMAW(0, i);
    tag = load_epoch(ws) + 1;
    if (job >= 0) kv_job<8>(ws, smem, job, w, lane);
#pragma unroll
    for (int i = 0; i < 4; ++i) G_DMAW(1, i);

    const int ga = 2 * wr + wn, gb = 2 * wr + 1 - wn;
    const int fx = (r >> 1) & 7;
    const int blk = (lane >> 4) & 1, q4 = (lane & 15) >> 2, p4 = lane & 3;
    const int wl0 = G_W + h * 4096 + wn * 1024 + q4 * 128 + (((blk * 2 + (p4 >> 1)) ^ (((q4 >> 1) & 1) << 2)) * 16) + (p4 & 1) * 8;
    const int wl1 = wl0 ^ 64;
    const int c0 = wl0 + wr * 2048, c1 = wl1 + wr * 2048, d0 = wl0 + (1 - wr) * 2048, d1 = wl1 + (1 - wr) * 2048;
    f32x16 ac0a = {}, ac0b = {}, ac1a = {}, ac1b = {}, ad0a = {}, ad0b = {}, ad1a = {}, ad1b = {};
    bf16x8 xa[2], xbf[2], fc0[2], fc1[2], fd0[2], fd1[2];
#define G_XS(u, j) (L + (((u) >> 1) & 1) * 16384 + r * 128 + (((2 * (((u) & 1) * 2 + (j)) + h) ^ fx) * 16))
#define G_WO(u, j) (((u) % 3) * 16384 + (j) * 8192)
#define G_READ(set, u, j)                                                             \
    do {                                                                              \
        fc0[set] = lds_tr2<512>(L + c0 + G_WO(u, j));                                 \
        xa[set] = lds_b128(G_XS(u, j) + ga * 4096);                                   \
        xbf[set] = lds_b128(G_XS(u, j) + gb * 4096);                                  \
        fc1[set] = lds_tr2<512>(L + c1 + G_WO(u, j));                                 \
        fd0[set] = lds_tr2<512>(L + d0 + G_WO(u, j));                                 \
        fd1[set] = lds_tr2<512>(L + d1 + G_WO(u, j));                                 \
        SBAR();                                                                       \
    } while (0)
#define G_STEP(set, RD, nu, nj, D0, D1, D2, D3)                                       \
    do {                                                                              \
        ac0a = MFMA(fc0[set], xa[set], ac0a); SBAR();                                 \
        if (RD) { fc0[1 - set] = lds_tr2<512>(L + c0 + G_WO(nu, nj)); SBAR(); }       \
        ac0b = MFMA(fc0[set], xbf[set], ac0b); SBAR();                                \
        if (RD) { xa[1 - set] = lds_b128(G_XS(nu, nj) + ga * 4096); xbf[1 - set] = lds_b128(G_XS(nu, nj) + gb * 4096); SBAR(); } \
        ac1a = MFMA(fc1[set], xa[set], ac1a); SBAR();                                 \
        if (RD) { fc1[1 - set] = lds_tr2<512>(L + c1 + G_WO(nu, nj)); SBAR(); }       \
        ac1b = MFMA(fc1[set], xbf[set], ac1b); SBAR();                                \
        if (RD) { fd0[1 - set] = lds_tr2<512>(L + d0 + G_WO(nu, nj)); SBAR(); }       \
        ad0a = MFMA(fd0[set], xa[set], ad0a); SBAR();                                 \
        if (RD) { fd1[1 - set] = lds_tr2<512>(L + d1 + G_WO(nu, nj)); SBAR(); }       \
        D2;                                                                           \
        ad0b = MFMA(fd0[set], xbf[set], ad0b); SBAR();                                \
        D0;                                                                           \
        ad1a = MFMA(fd1[set], xa[set], ad1a); SBAR();                                 \
        D1;                                                                           \
        ad1b = MFMA(fd1[set], xbf[set], ad1b); SBAR();                                \
        D3;                                                                           \
    } while (0)
    STAMP(1, 1);
    wait_bar<4>();
    STAMP(1, 2);
    if (job >= 0 && threadIdx.x == 0) set_flag(ws, SY_KV + (size_t)job * FL, tag);
    __builtin_amdgcn_s_setprio(G1PRIO);
    if (lane == 0 && w == 1) set_flag(ws, SY_SEEN + (size_t)(256 + bid) * 8, tag);
    G_READ(0, 0, 0);
    G_DMAW(2, 0); G_DMAW(2, 1); G_DMAX(1, 0); G_DMAX(1, 1);
#pragma unroll
    for (int u = 0; u < 16; ++u) {
        if ((u & 1) == 0) G_STEP(0, true, u, 1, G_DMAW(u + 2, 2), G_DMAW(u + 2, 3), G_DMAX(u / 2 + 1, 2), G_DMAX(u / 2 + 1, 3));
        else              G_STEP(0, true, u, 1, G_DMAW(u + 2, 2), G_DMAW(u + 2, 3), (void)0, (void)0);
        if (u < 15) {
            const int nfly = (u + 2 < 16 ? 4 : 0) + ((((u & 1) == 0) && (u / 2 + 1 < 8)) ? 4 : 0);
            if (nfly == 8) wait_bar<8>(); else if (nfly == 4) wait_bar<4>(); else wait_bar<0>();
        }
        if (u & 1) G_STEP(1, u < 15, u + 1, 0, G_DMAW(u + 3, 0), G_DMAW(u + 3, 1), G_DMAX((u + 1) / 2 + 1, 0), G_DMAX((u + 1) / 2 + 1, 1));
        else       G_STEP(1, true, u + 1, 0, G_DMAW(u + 3, 0), G_DMAW(u + 3, 1), (void)0, (void)0);
    }
#undef G_STEP
#undef G_XS
#undef G_WO
#undef G_READ
#undef G_DMAX
#undef G_DMAW
    STAMP(1, 3);
    wait_flags(ws, (lane & 2) ? (unsigned)(SY_C + (cg + 14 * (lane & 1)) * FL) : (unsigned)(SY_KV + (rt * 4 + 2 * wr + (lane & 1)) * 2 * FL), tag);
    const __amdgpu_buffer_rsrc_t rst = __builtin_amdgcn_make_buffer_rsrc(ws + WS_STAT, 0, 32768, 0x00020000);
    const u32x2 sta = __builtin_amdgcn_raw_buffer_load_b64(rst, rt * 1024 + ga * 256 + r * 8, 0, SC1);
    const u32x2 stb = __builtin_amdgcn_raw_buffer_load_b64(rst, rt * 1024 + gb * 256 + r * 8, 0, SC1);
    const unsigned co_c = 16384 + ((wr * 14 + cg) * 128 + wn * 64 + r) * 4, co_d = 16384 + (((1 - wr) * 14 + cg) * 128 + wn * 64 + r) * 4;
    const float csc0 = __uint_as_float(__builtin_amdgcn_raw_buffer_load_b32(rst, co_c, 0, SC1));
    const float csc1 = __uint_as_float(__builtin_amdgcn_raw_buffer_load_b32(rst, co_c + 128, 0, SC1));
    const float csd0 = __uint_as_float(__builtin_amdgcn_raw_buffer_load_b32(rst, co_d, 0, SC1));
    const float csd1 = __uint_as_float(__builtin_amdgcn_raw_buffer_load_b32(rst, co_d + 128, 0, SC1));
    const float mu_a = __uint_as_float(sta[0]), rs_a = __uint_as_float(sta[1]), mu_b = __uint_as_float(stb[0]), rs_b = __uint_as_float(stb[1]);
    {
        const bf16x8 ea = xext(mu_a, h), eb = xext(mu_b, h);
        const bf16x8 ec0 = wext(csc0, h), ec1 = wext(csc1, h), ed0 = wext(csd0, h), ed1 = wext(csd1, h);
        ac0a = MFMA(ec0, ea, ac0a); ad0a = MFMA(ed0, ea, ad0a);
        ac0b = MFMA(ec0, eb, ac0b); ad0b = MFMA(ed0, eb, ad0b);
        ac1a = MFMA(ec1, ea, ac1a); ad1a = MFMA(ed1, ea, ad1a);
        ac1b = MFMA(ec1, eb, ac1b); ad1b = MFMA(ed1, eb, ad1b);
    }
    const __amdgpu_buffer_rsrc_t rh = __builtin_amdgcn_make_buffer_rsrc(
        ws + WS_ACT + ((size_t)rt * 4 * NKS + 32 + 8 * cg + 4 * wn) * 1024, 0, 4 * NKS * 1024, 0x00020000);
    const float K1c = 2.0f * 0.7978845608028654f * 1.4426950408889634f, K3c = K1c * 0.044715f;
    auto geglu = [&](const f32x16& G, const f32x16& U, float rs, int so) {
        typedef float f32x2 __attribute__((ext_vector_type(2)));
        const float c1_ = -K1c * rs, c3_ = -K3c * rs * rs * rs, ri2 = __builtin_amdgcn_rcpf(rs * rs);
        const f32x2 c1v = {c1_, c1_}, c3v = {c3_, c3_}, riv = {ri2, ri2};
        u32x4 o0, o1;
#pragma unroll
        for (int i = 0; i < 8; ++i) {
            const f32x2 gg = {G[2 * i], G[2 * i + 1]}, uu = {U[2 * i], U[2 * i + 1]};
            const f32x2 t = gg * __builtin_elementwise_fma(gg * gg, c3v, c1v);
            const f32x2 e = {EXP2(t[0]), EXP2(t[1])};
            const f32x2 d = __builtin_elementwise_fma(e, riv, riv);
            const f32x2 rr = {__builtin_amdgcn_rcpf(d[0]), __builtin_amdgcn_rcpf(d[1])};
            const f32x2 hv = (gg * uu) * rr;
            const unsigned pk = cvtpk(hv[0], hv[1]);
            if (i < 4) o0[i] = pk; else o1[i - 4] = pk;
        }
        __builtin_amdgcn_raw_buffer_store_b128(o0, rh, lo16 + so, 0, SC1);
        __builtin_amdgcn_raw_buffer_store_b128(o1, rh, lo16 + so + 1024, 0, SC1);
    };
    const int soa = ga * NKS * 1024, sob = gb * NKS * 1024;
    const int nlow = job >= 0 ? NLOWJ : NLOWN;
#define GE_PRIO(i) do { if (nlow == (i)) __builtin_amdgcn_s_setprio(2); } while (0)
#define GE_HALF()                                                                                        \
    do {                                                                                                 \
        asm volatile("s_waitcnt vmcnt(2)\n\ts_barrier" ::: "memory");     \
        if (threadIdx.x == 0) set_flag(ws, SY_H + (size_t)(rt * 16 + cg) * FL, tag);                     \
    } while (0)
    if (wr == 0) {
        GE_PRIO(0); geglu(ac0a, ad0a, rs_a, soa);        GE_PRIO(1); geglu(ac0b, ad0b, rs_b, sob);
        GE_PRIO(2); geglu(ac1a, ad1a, rs_a, soa + 2048); GE_HALF(); GE_PRIO(3); geglu(ac1b, ad1b, rs_b, sob + 2048);
    } else {
        GE_PRIO(0); geglu(ad0a, ac0a, rs_a, soa);        GE_PRIO(1); geglu(ad0b, ac0b, rs_b, sob);
        GE_PRIO(2); geglu(ad1a, ac1a, rs_a, soa + 2048); GE_HALF(); GE_PRIO(3); geglu(ad1b, ac1b, rs_b, sob + 2048);
    }
#undef GE_HALF
#undef GE_PRIO
    asm volatile("s_waitcnt vmcnt(0) lgkmcnt(0)\n\ts_barrier" ::: "memory");
    if (threadIdx.x == 0) set_flag(ws, SY_H + (size_t)(rt * 16 + cg) * FL + 64, tag);
}

DI void g_retile(const u16* __restrict__ wo, const u16* __restrict__ wd, const u16* __restrict__ wg, const u16* __restrict__ wu,
                 char* __restrict__ ws, char* smem, const int bid,
                 const int j, const int job, const int w, const int lane, u64& tag) {
    const int r = lane & 31, h = lane >> 5;
    const int ng = j >> 1, ks0 = 72 * (j & 1) + 18 * w;
    const __amdgpu_buffer_rsrc_t rw =
        __builtin_amdgcn_make_buffer_rsrc(ws + WS_W2F + ((size_t)ng * NKS + ks0) * 1024, 0, 18 * 1024, 0x00020000);
    tag = load_epoch(ws) + 1;
    kv_job<0>(ws, smem, job, w, lane);
    if (w == 0) {
        asm volatile("s_waitcnt vmcnt(0)" ::: "memory");
        if (lane == 0) set_flag(ws, SY_KV + (size_t)job * FL, tag);
    }
    if (lane == 0 && w == 1) set_flag(ws, SY_SEEN + (size_t)(256 + bid) * 8, tag);
    if (j < 28) {
        const int t = threadIdx.x;
        const char* src = (const char*)((j < 14 ? wg : wu) + (j < 14 ? j : j - 14) * 128) + (size_t)(t >> 4) * 32 * (DF * 2) + (t & 15) * 16;
        float cs[8] = {0.f, 0.f, 0.f, 0.f, 0.f, 0.f, 0.f, 0.f};
#pragma unroll 1
        for (int b = 0; b < 2; ++b) {
            u32x4 v[16];
#pragma unroll
            for (int i = 0; i < 16; ++i) v[i] = *(const u32x4*)(src + (size_t)(b * 16 + i) * (DF * 2));
#pragma unroll
            for (int i = 0; i < 16; ++i)
#pragma unroll
                for (int d = 0; d < 4; ++d) {
                    const unsigned u = v[i][d];
                    cs[2 * d] += __uint_as_float(u << 16);
                    cs[2 * d + 1] += __uint_as_float(u & 0xffff0000u);
                }
        }
        *(f32x4*)(smem + t * 32) = (f32x4){cs[0], cs[1], cs[2], cs[3]};
        *(f32x4*)(smem + t * 32 + 16) = (f32x4){cs[4], cs[5], cs[6], cs[7]};
        __syncthreads();
        if (w == 0) {
            float s0 = 0.f, s1 = 0.f;
#pragma unroll
            for (int k = 0; k < 16; ++k) { s0 += ((const float*)smem)[k * 128 + 2 * lane]; s1 += ((const float*)smem)[k * 128 + 2 * lane + 1]; }
            const __amdgpu_buffer_rsrc_t rc = __builtin_amdgcn_make_buffer_rsrc(ws + WS_CS + j * 512, 0, 512, 0x00020000);
            __builtin_amdgcn_raw_buffer_store_b64((u32x2){__float_as_uint(s0), __float_as_uint(s1)}, rc, lane * 8, 0, SC1);
            asm volatile("s_waitcnt vmcnt(0)" ::: "memory");
            if (lane == 0) set_flag(ws, SY_C + (size_t)j * FL, tag);
        }
    }
#pragma unroll 1
    for (int b = 0; b < 3; ++b) {
        u16 v[6][8];
#pragma unroll
        for (int f = 0; f < 6; ++f) {
            const int ks = ks0 + b * 6 + f;
            const u16* src = (ks < 32 ? wo + (size_t)ks * 16 * DM : wd + (size_t)(ks - 32) * 16 * DM) + ng * 32 + r + h * 4 * DM;
#pragma unroll
            for (int e = 0; e < 8; ++e) v[f][e] = src[(8 * (e >> 2) + (e & 3)) * DM];
        }
#pragma unroll
        for (int f = 0; f < 6; ++f) {
            u32x4 o;
#pragma unroll
            for (int i = 0; i < 4; ++i) o[i] = (unsigned)v[f][2 * i] | ((unsigned)v[f][2 * i + 1] << 16);
            __builtin_amdgcn_raw_buffer_store_b128(o, rw, lane * 16 + (b * 6 + f) * 1024, 0, SC1);
        }
    }
    asm volatile("s_waitcnt vmcnt(0)\n\ts_barrier" ::: "memory");
    if (threadIdx.x == 0) set_flag(ws, SY_W + (size_t)j * FL, tag);
    if (j == 0 && w == 0) {
#pragma unroll 1
        for (int i = 0; i < 8; ++i) wait_flags(ws, SY_SEEN + i * 512 + lane * 8, tag);
        if (lane == 0) set_flag(ws, SY_EPOCH, tag);
    }
}

DI void role_g(const u16* __restrict__ x, const u16* __restrict__ wk, const u16* __restrict__ wv, const u16* __restrict__ wo, const u16* __restrict__ wg, const u16* __restrict__ wu,
               const u16* __restrict__ wd, char* __restrict__ ws, u16* __restrict__ out, char* smem, const int bid, const int w,
               const int lane) {
    const int xcd = bid & 7, idx = bid >> 3;
    const int rt = (xcd & 1) * 8 + (idx & 7), cg = (xcd >> 1) * 4 + (idx >> 3);
    const int it = xcd * 4 + (idx & 3), g = idx >> 2;
    const unsigned lo16 = lane * 16;
    u64 tag;
    __builtin_amdgcn_s_setprio(3);
    STAMP(1, 0);
    const int job = idx >= 16 ? xcd * 16 + (idx - 16) : -1;
    if (job >= 0) kv_issue(x, (job & 1) ? wv : wk, smem, job, w, lane);
    if (cg < 14) g_gateup(x, wg, wu, ws, smem, bid, rt, cg, job, w, lane, tag);
    else g_retile(wo, wd, wg, wu, ws, smem, bid, rt * 2 + (cg - 14), job, w, lane, tag);
    STAMP(1, 4);
    __builtin_amdgcn_s_setprio(G2PRIO);

    const unsigned hfo = (unsigned)(SY_H + ((it >> 1) * 16 + ((7 * w) >> 1) + (lane & 3)) * FL);
    wait_flags(ws, lane < 8 ? hfo : (unsigned)(SY_W + (4 * g + (lane & 3)) * FL), tag);
    STAMP(1, 5);
    const __amdgpu_buffer_rsrc_t ra =
        __builtin_amdgcn_make_buffer_rsrc(ws + WS_ACT + (size_t)(2 * it) * NKS * 1024, 0, 2 * NKS * 1024, 0x00020000);
    const __amdgpu_buffer_rsrc_t rw =
        __builtin_amdgcn_make_buffer_rsrc(ws + WS_W2F + (size_t)(2 * g) * NKS * 1024, 0, 2 * NKS * 1024, 0x00020000);
    const int so = (32 + 28 * w) * 1024;
    constexpr int PD = G2PD;
    f32x16 acc00 = {}, acc01 = {}, acc10 = {}, acc11 = {};
    u32x4 fw0[8], fw1[8], fa0[8], fa1[8];
#define G_LOAD(i, k)                                                                                   \
    do {                                                                                               \
        fw0[i] = __builtin_amdgcn_raw_buffer_load_b128(rw, lo16, so + (k) * 1024, SC1);                \
        fa0[i] = __builtin_amdgcn_raw_buffer_load_b128(ra, lo16, so + (k) * 1024, SC1);                \
        fa1[i] = __builtin_amdgcn_raw_buffer_load_b128(ra, lo16, so + (NKS + (k)) * 1024, SC1);        \
        fw1[i] = __builtin_amdgcn_raw_buffer_load_b128(rw, lo16, so + (NKS + (k)) * 1024, SC1);        \
        SBAR();                                                                                        \
    } while (0)
#define G_MMA4(i)                                                                                      \
    do {                                                                                               \
        acc00 = MFMA(BC8(fw0[i]), BC8(fa0[i]), acc00); acc01 = MFMA(BC8(fw0[i]), BC8(fa1[i]), acc01);  \
        acc10 = MFMA(BC8(fw1[i]), BC8(fa0[i]), acc10); acc11 = MFMA(BC8(fw1[i]), BC8(fa1[i]), acc11);  \
        SBAR();                                                                                        \
    } while (0)
#define KORD(n) ((n) < 14 ? ((n) >> 1) * 4 + ((n) & 1) : (((n) - 14) >> 1) * 4 + 2 + ((n) & 1))
#pragma unroll
    for (int i = 0; i < PD; ++i) G_LOAD(i, KORD(i));
#pragma unroll
    for (int k = 0; k < 28; ++k) {
        G_MMA4(k % PD);
        if (k + PD == 14) wait_flags(ws, hfo + 64, tag);
        if (k + PD < 28) G_LOAD(k % PD, KORD(k + PD));
    }
#undef KORD
#pragma unroll
    for (int i = 0; i < 8; ++i) {
        fw0[i] = __builtin_amdgcn_raw_buffer_load_b128(rw, lo16, (8 * w + i) * 1024, SC1);
        fw1[i] = __builtin_amdgcn_raw_buffer_load_b128(rw, lo16, (NKS + 8 * w + i) * 1024, SC1);
    }
    u32x4 xv[2];
#pragma unroll
    for (int u = 0; u < 2; ++u) {
        const int t2 = threadIdx.x + 256 * u;
        xv[u] = *(const u32x4*)(x + (size_t)(it * 64 + (t2 >> 3)) * DM + g * 64 + (t2 & 7) * 8);
    }
    STAMP(1, 6);
    wait_flags(ws, (unsigned)(SY_O + (it * 8 + 2 * w + (lane & 1)) * FL), tag);
    STAMP(1, 7);
    __builtin_amdgcn_s_setprio(2);
#pragma unroll
    for (int i = 0; i < 8; ++i) {
        fa0[i] = __builtin_amdgcn_raw_buffer_load_b128(ra, lo16, (8 * w + i) * 1024, SC1);
        fa1[i] = __builtin_amdgcn_raw_buffer_load_b128(ra, lo16, (NKS + 8 * w + i) * 1024, SC1);
    }
    SBAR();
#pragma unroll
    for (int i = 0; i < 8; ++i) G_MMA4(i);
#undef G_LOAD
#undef G_MMA4
    char* dst = smem + w * 16640 + lo16;
#pragma unroll
    for (int q = 0; q < 4; ++q) {
        *(f32x4*)(dst + (0 + q) * 1040) = (f32x4){acc00[4 * q], acc00[4 * q + 1], acc00[4 * q + 2], acc00[4 * q + 3]};
        *(f32x4*)(dst + (4 + q) * 1040) = (f32x4){acc01[4 * q], acc01[4 * q + 1], acc01[4 * q + 2], acc01[4 * q + 3]};
        *(f32x4*)(dst + (8 + q) * 1040) = (f32x4){acc10[4 * q], acc10[4 * q + 1], acc10[4 * q + 2], acc10[4 * q + 3]};
        *(f32x4*)(dst + (12 + q) * 1040) = (f32x4){acc11[4 * q], acc11[4 * q + 1], acc11[4 * q + 2], acc11[4 * q + 3]};
    }
    STAMP(1, 8);
    __syncthreads();
#pragma unroll
    for (int u = 0; u < 2; ++u) {
        const int t2 = threadIdx.x + 256 * u, row = t2 >> 3, c8 = t2 & 7;
        const int a = c8 >> 2, q = c8 & 3, b = row >> 5, rr = row & 31;
        const char* s0 = smem + ((a * 2 + b) * 4 + q) * 1040 + rr * 16;
        f32x4 lo = {__uint_as_float(xv[u][0] << 16), __uint_as_float(xv[u][0] & 0xffff0000u), __uint_as_float(xv[u][1] << 16), __uint_as_float(xv[u][1] & 0xffff0000u)};
        f32x4 hi = {__uint_as_float(xv[u][2] << 16), __uint_as_float(xv[u][2] & 0xffff0000u), __uint_as_float(xv[u][3] << 16), __uint_as_float(xv[u][3] & 0xffff0000u)};
#pragma unroll
        for (int gw = 0; gw < 4; ++gw) {
            lo += *(const f32x4*)(s0 + gw * 16640);
            hi += *(const f32x4*)(s0 + gw * 16640 + 512);
        }
        u32x4 o = {cvtpk(lo[0], lo[1]), cvtpk(lo[2], lo[3]), cvtpk(hi[0], hi[1]), cvtpk(hi[2], hi[3])};
        *(u32x4*)(out + (size_t)(it * 64 + row) * DM + g * 64 + c8 * 8) = o;
    }
    STAMP(1, 9);
}

__global__ __launch_bounds__(256, 2) void fused_block(const u16* __restrict__ x, const u16* __restrict__ wq,
                                                      const u16* __restrict__ wk, const u16* __restrict__ wv,
                                                      const u16* __restrict__ wo, const u16* __restrict__ wg,
                                                      const u16* __restrict__ wu, const u16* __restrict__ wd,
                                                      char* __restrict__ ws, u16* __restrict__ out) {
    __shared__ __attribute__((aligned(16))) char smem[81920];
    const int lane = threadIdx.x & 63;
    const int w = __builtin_amdgcn_readfirstlane(threadIdx.x >> 6);
    if (blockIdx.x < 256) role_a(x, wq, ws, smem, blockIdx.x, w, lane);
    else role_g(x, wk, wv, wo, wg, wu, wd, ws, out, smem, blockIdx.x - 256, w, lane);
}

extern "C" void kernel_launch(void* const* d_in, const int* in_sizes, int n_in, void* d_out, int out_size, void* d_ws,
                              size_t ws_size, hipStream_t stream) {
    fused_block<<<512, 256, 0, stream>>>((const u16*)d_in[0], (const u16*)d_in[1], (const u16*)d_in[2], (const u16*)d_in[3],
                                         (const u16*)d_in[4], (const u16*)d_in[6], (const u16*)d_in[7], (const u16*)d_in[8],
                                         (char*)d_ws, (u16*)d_out);
}
```

```cpp
#include <hip/hip_runtime.h>
#include <hip/hip_bf16.h>
#include <stdint.h>

typedef __bf16 bf16x8 __attribute__((ext_vector_type(8)));
typedef __bf16 bf16x2 __attribute__((ext_vector_type(2)));
typedef short s16x4 __attribute__((ext_vector_type(4)));
typedef float f32x16 __attribute__((ext_vector_type(16)));
typedef float f32x4 __attribute__((ext_vector_type(4)));
typedef unsigned u32x4 __attribute__((ext_vector_type(4)));
typedef unsigned u32x2 __attribute__((ext_vector_type(2)));
typedef unsigned short u16;
typedef unsigned long long u64;
typedef __attribute__((address_space(1))) u64 gu64;
#define LAS __attribute__((address_space(3)))
#define DI __device__ __forceinline__
#define MFMA(a, b, c) __builtin_amdgcn_mfma_f32_32x32x16_bf16((a), (b), (c), 0, 0, 0)
#define MFMA16(a, b, c) __builtin_amdgcn_mfma_f32_16x16x32_bf16((a), (b), (c), 0, 0, 0)
#define SBAR() __builtin_amdgcn_sched_barrier(0)
#define EXP2(v) __builtin_amdgcn_exp2f(v)
#define BC8(v) __builtin_bit_cast(bf16x8, v)
#define SC1 16

constexpr int DM = 512, DF = 1792;
constexpr int NKS = 144;
constexpr size_t WS_VF = 65536;
constexpr size_t WS_STAT = 131072;
constexpr size_t WS_CS = 131072 + 16384;
constexpr size_t WS_SYNC = 131072 + 32768;
constexpr size_t SY_EPOCH = 0, SY_SEEN = 4096, SY_W = 8192, SY_C = 12288, SY_KV = 16384, SY_O = 32768, SY_H = 65536;
constexpr int FL = 128;
constexpr size_t WS_ACT = 262144;
constexpr size_t WS_W2F = WS_ACT + (size_t)64 * NKS * 1024;
constexpr unsigned SPIN_CAP = 1u << 18;


DI unsigned cvtpk(float lo, float hi) {
    bf16x2 r = {(__bf16)lo, (__bf16)hi};
    return __builtin_bit_cast(unsigned, r);
}
DI float bf_round(float v) { return (float)(__bf16)v; }
DI float swap_add(float v) {
    auto rr = __builtin_amdgcn_permlane32_swap(__float_as_uint(v), __float_as_uint(v), false, false);
    return __uint_as_float(rr[0]) + __uint_as_float(rr[1]);
}
DI float swap_max(float v) {
    auto rr = __builtin_amdgcn_permlane32_swap(__float_as_uint(v), __float_as_uint(v), false, false);
    return __builtin_fmaxf(__uint_as_float(rr[0]), __uint_as_float(rr[1]));
}
DI void glds16s(const void* sbase, unsigned voff, unsigned lds_base) {
    asm volatile("s_mov_b32 m0, %2\n\ts_nop 0\n\tglobal_load_lds_dwordx4 %0, %1" ::"v"(voff), "s"(sbase), "s"(lds_base) : "memory");
    SBAR();
}
template <int N> DI void wait_bar() { asm volatile("s_waitcnt vmcnt(%0) lgkmcnt(0)\n\ts_barrier" ::"n"(N) : "memory"); }
DI bf16x8 lds_b128(LAS const char* p) { return *(LAS const bf16x8*)p; }
template <int STEP> DI bf16x8 lds_tr2(LAS const char* p) {
    s16x4 lo = __builtin_amdgcn_ds_read_tr16_b64_v4i16((LAS s16x4*)p);
    s16x4 hi = __builtin_amdgcn_ds_read_tr16_b64_v4i16((LAS s16x4*)(p + STEP));
    return __builtin_bit_cast(bf16x8, __builtin_shufflevector(lo, hi, 0, 1, 2, 3, 4, 5, 6, 7));
}
DI u32x4 pack8(const f32x16& v, int o, float s) {
    u32x4 r;
#pragma unroll
    for (int i = 0; i < 4; ++i) r[i] = cvtpk(v[o + 2 * i] * s, v[o + 2 * i + 1] * s);
    return r;
}
DI void ln_stat(float sum, float sq, float& mu, float& rs) {
    const float mean = sum * (1.0f / DM);
    const float var = __builtin_fmaxf(sq * (1.0f / DM) - mean * mean, 0.f);
    const float sd = bf_round(__builtin_amdgcn_sqrtf(bf_round(bf_round(var) + 1e-5f)));
    mu = bf_round(mean); rs = __builtin_amdgcn_rcpf(sd);
}
DI bf16x8 wext(float cs, int h) {
    const float hi = bf_round(cs);
    u32x4 v = {h == 0 ? cvtpk(hi, cs - hi) : 0u, 0u, 0u, 0u};
    return BC8(v);
}
DI bf16x8 xext(float mu, int h) {
    u32x4 v = {h == 0 ? cvtpk(-mu, -mu) : 0u, 0u, 0u, 0u};
    return BC8(v);
}
DI bf16x8 make_sel(int lane) {
    const unsigned o1 = ((lane & 3) == 0 && ((lane >> 2) & 1) == ((lane >> 4) & 1)) ? 0x3F803F80u : 0u;
    return BC8(((u32x4){o1, o1, o1, o1}));
}
DI float diag32(const f32x16& a, int r) {
    typedef float f32x8 __attribute__((ext_vector_type(8)));
    typedef float f32x2 __attribute__((ext_vector_type(2)));
    const f32x8 t8 = (r & 16) ? __builtin_shufflevector(a, a, 8, 9, 10, 11, 12, 13, 14, 15)
                              : __builtin_shufflevector(a, a, 0, 1, 2, 3, 4, 5, 6, 7);
    const f32x4 t4 = (r & 8) ? __builtin_shufflevector(t8, t8, 4, 5, 6, 7) : __builtin_shufflevector(t8, t8, 0, 1, 2, 3);
    const f32x2 t2 = (r & 2) ? __builtin_shufflevector(t4, t4, 2, 3) : __builtin_shufflevector(t4, t4, 0, 1);
    const float t20 = t2[0], t21 = t2[1];
    return (r & 1) ? t21 : t20;
}
DI u64 load_epoch(const char* ws) {
    u64 e;
    asm volatile("s_load_dwordx2 %0, %1, 0x0\n\ts_waitcnt lgkmcnt(0)" : "=s"(e) : "s"(ws + WS_SYNC + SY_EPOCH) : "memory");
    return e;
}
DI void set_flag(char* ws, size_t off, u64 tag) {
    __hip_atomic_store((gu64*)(ws + WS_SYNC + off), tag, __ATOMIC_RELAXED, __HIP_MEMORY_SCOPE_AGENT);
}
DI void wait_flags(char* ws, unsigned off, u64 tag) {
    gu64* p = (gu64*)(ws + WS_SYNC + off);
    for (unsigned spins = 0; spins < SPIN_CAP; ++spins) {
        const u64 v = __hip_atomic_load(p, __ATOMIC_RELAXED, __HIP_MEMORY_SCOPE_AGENT);
        if (__all(v == tag)) break;
        __builtin_amdgcn_s_sleep(2);
    }
    __builtin_amdgcn_fence(__ATOMIC_ACQUIRE, "wavefront");
    asm volatile("" ::: "memory");
}
DI float max16(const f32x16& s) {
    float a = __builtin_fmaxf(s[0], s[1]);
#pragma unroll
    for (int i = 2; i < 16; i += 2) a = __builtin_fmaxf(__builtin_fmaxf(a, s[i]), s[i + 1]);
    return a;
}

DI f32x16 attn_slow(const __amdgpu_buffer_rsrc_t rkv, const bf16x8 q, const int lane) {
    const int r = lane & 31, h = lane >> 5;
    const unsigned vo = (unsigned)WS_VF + h * 256 + (r & 15) * 16;
    float m = -3.0e38f;
    f32x16 o = {};
#pragma unroll 1
    for (int t = 0; t < 64; ++t) {
        const u32x4 kf = __builtin_amdgcn_raw_buffer_load_b128(rkv, lane * 16, t * 1024, SC1);
        u32x4 v0 = __builtin_amdgcn_raw_buffer_load_b128(rkv, vo, t * 1024, SC1);
        u32x4 v1 = __builtin_amdgcn_raw_buffer_load_b128(rkv, vo + 512, t * 1024, SC1);
        if (r >= 16) { const unsigned c_ = r == 16 ? 0x3F803F80u : 0u; v0 = (u32x4){c_, c_, c_, c_}; v1 = v0; }
        f32x16 z = {};
        f32x16 s = MFMA(BC8(kf), q, z);
        const float mn = __builtin_fmaxf(m, swap_max(max16(s)));
        const float al = EXP2(m - mn);
        m = mn;
#pragma unroll
        for (int i = 0; i < 16; ++i) { s[i] -= mn; o[i] *= al; }
        u32x4 p0, p1;
#pragma unroll
        for (int i = 0; i < 4; ++i) {
            p0[i] = cvtpk(EXP2(s[2 * i]), EXP2(s[2 * i + 1]));
            p1[i] = cvtpk(EXP2(s[8 + 2 * i]), EXP2(s[8 + 2 * i + 1]));
        }
        o = MFMA(BC8(v0), BC8(p0), o);
        o = MFMA(BC8(v1), BC8(p1), o);
    }
    return o;
}

constexpr int A_STAGE = 16384;
constexpr int VT = 1152, VH = 288;
constexpr int A_SLOT = 8 * 1024 + 8 * VT;
DI void role_a(const u16* __restrict__ x, const u16* __restrict__ wq, char* __restrict__ ws, char* smem, const int bid, const int w, const int lane) {
    const int xcd = bid & 7, idx = bid >> 3;
    const int it = xcd * 4 + (idx & 3), g = idx >> 2;
    const int p = w >> 1, rgl = w & 1;
    const int r = lane & 31, h = lane >> 5;
    const unsigned lds0 = (unsigned)(uintptr_t)smem;
    const unsigned lo16 = lane * 16;
    LAS const char* L = (LAS const char*)smem;
    __builtin_amdgcn_s_setprio(2);
    for (int i_ = 0; i_ < 9; ++i_) __builtin_amdgcn_s_sleep(8);

    const int prow = lane >> 3, pslot = lane & 7;
    const unsigned xo0 = (unsigned)((16 * w + prow) * DM * 2 + ((pslot ^ (((16 * w + prow) >> 1) & 7)) * 16));
    const unsigned xo1 = (unsigned)((16 * w + 8 + prow) * DM * 2 + ((pslot ^ (((16 * w + 8 + prow) >> 1) & 7)) * 16));
    const unsigned wo0 = (unsigned)((16 * w + prow) * DM * 2 + ((pslot ^ (((prow >> 1) & 1) << 2)) * 16));
    const unsigned wo1 = wo0 + 8 * DM * 2;
    const char* xb = (const char*)(x + (size_t)it * 64 * DM);
    const char* wb = (const char*)(wq + g * 64);
    const int blk = (lane >> 4) & 1, q4 = (lane & 15) >> 2, p4 = lane & 3;
    const bf16x8 sel = make_sel(lane);
#define A_DMA(s, i)                                                                                       \
    do {                                                                                                  \
        if ((s) < 8) {                                                                                    \
            const unsigned sb_ = lds0 + ((s) & 3) * A_STAGE + w * 2048;                                   \
            if ((i) == 0) glds16s(xb + (s) * 128, xo0, sb_);                                              \
            if ((i) == 1) glds16s(xb + (s) * 128, xo1, sb_ + 1024);                                       \
            if ((i) == 2) glds16s(wb + (s) * (64 * DM * 2), wo0, sb_ + 8192);                             \
            if ((i) == 3) glds16s(wb + (s) * (64 * DM * 2), wo1, sb_ + 8192 + 1024);                      \
        }                                                                                                 \
    } while (0)
#define A_DMA4(s) do { A_DMA(s, 0); A_DMA(s, 1); A_DMA(s, 2); A_DMA(s, 3); } while (0)
    A_DMA4(0); A_DMA4(1); A_DMA4(2); A_DMA4(3);
    const u64 tag = load_epoch(ws) + 1;

    const int fx = (r >> 1) & 7;
    const int wlane = 8192 + h * 1024 + q4 * 128 + (((p * 4 + blk * 2 + (p4 >> 1)) ^ (((q4 >> 1) & 1) << 2)) * 16) + (p4 & 1) * 8;
    f32x16 accq = {};
    f32x4 acc_cs = {};
    bf16x8 xa[2], wc[2];
#define A_READ(set, s, j)                                                             \
    do {                                                                              \
        LAS const char* sb_ = L + ((s) & 3) * A_STAGE;                                \
        wc[set] = lds_tr2<512>(sb_ + wlane + (j) * 2048);                             \
        xa[set] = lds_b128(sb_ + (rgl * 32 + r) * 128 + (((2 * (j) + h) ^ fx) * 16)); \
        SBAR();                                                                       \
    } while (0)
#define A_MMA(set, j, ds, d0)                                                         \
    do {                                                                              \
        accq = MFMA(wc[set], xa[set], accq);                                          \
        acc_cs = MFMA16(sel, wc[set], acc_cs);                                        \
        SBAR();                                                                       \
        A_DMA(ds, d0);                                                                \
    } while (0)
    wait_bar<12>();
    if (lane == 0 && w == 1) set_flag(ws, SY_SEEN + (size_t)bid * 8, tag);
    A_READ(0, 0, 0);
#pragma unroll
    for (int s = 0; s < 8; ++s) {
        A_READ(1, s, 1); A_MMA(0, 0, (s >= 1 ? s + 3 : 8), 1);
        A_READ(0, s, 2); A_MMA(1, 1, (s >= 1 ? s + 3 : 8), 2);
        A_READ(1, s, 3); A_MMA(0, 2, (s >= 1 ? s + 3 : 8), 3);
        if (s < 7) {
            if (s <= 4) wait_bar<8>(); else if (s == 5) wait_bar<4>(); else wait_bar<0>();
            A_READ(0, s + 1, 0);
        }
        A_MMA(1, 3, s + 4, 0);
    }
#undef A_READ
#undef A_MMA
#undef A_DMA
#undef A_DMA4

    const __amdgpu_buffer_rsrc_t rkv = __builtin_amdgcn_make_buffer_rsrc(ws, 0, 131072, 0x00020000);
    gu64* kvf = (gu64*)(ws + WS_SYNC + SY_KV +
                        (size_t)(lane < 32 ? ((lane >> 2) * 8 + 2 * w) * 2 + (lane & 3) : (2 * it + rgl) * 2) * FL);
    unsigned long long rdy = 0;
    for (unsigned spins = 0; spins < SPIN_CAP; ++spins) {
        rdy = __ballot(__hip_atomic_load(kvf, __ATOMIC_RELAXED, __HIP_MEMORY_SCOPE_AGENT) == tag);
        if ((rdy & 0x100000FFFull) == 0x100000FFFull) break;
        __builtin_amdgcn_s_sleep(1);
    }
    __builtin_amdgcn_fence(__ATOMIC_ACQUIRE, "wavefront");
    asm volatile("" ::: "memory");
    bf16x8 qa, qb;
    const __amdgpu_buffer_rsrc_t rst = __builtin_amdgcn_make_buffer_rsrc(ws + WS_STAT + (2 * it + rgl) * 256, 0, 256, 0x00020000);
    const u32x2 stq = __builtin_amdgcn_raw_buffer_load_b64(rst, r * 8, 0, SC1);
    u32x4 sk0, sk1, sv0, sv1;
#define KV_LOAD(c)                                                                                     \
    do {                                                                                               \
        const int so_ = ((c) * 8 + 2 * w) * 1024;                                                      \
        sk0 = __builtin_amdgcn_raw_buffer_load_b128(rkv, lo16, so_, SC1);                              \
        sk1 = __builtin_amdgcn_raw_buffer_load_b128(rkv, lo16 + 1024, so_, SC1);                       \
        sv0 = __builtin_amdgcn_raw_buffer_load_b128(rkv, lo16 + (unsigned)WS_VF, so_, SC1);            \
        sv1 = __builtin_amdgcn_raw_buffer_load_b128(rkv, lo16 + (unsigned)WS_VF + 1024, so_, SC1);     \
    } while (0)
    const int kwr = w * 2048 + lo16, vwr = 8192 + w * 2 * VT + (lane >> 4) * VH + (lane & 15) * 16;
#define KV_WRITE(slotbase)                                                                             \
    do {                                                                                               \
        *(LAS u32x4*)(slotbase + kwr) = sk0; *(LAS u32x4*)(slotbase + kwr + 1024) = sk1;               \
        *(LAS u32x4*)(slotbase + vwr) = sv0; *(LAS u32x4*)(slotbase + vwr + VT) = sv1;                 \
    } while (0)
    LAS char* LW = (LAS char*)smem;
#define KV_CONST(s3)                                                                                   \
    do {                                                                                               \
        const unsigned c_ = (lane & 32) ? 0u : 0x3F803F80u;                                            \
        *(LAS u32x4*)(LW + (s3) * A_SLOT + 8192 + (lane & 31) * VH + 256 + ((lane & 32) >> 1)) = (u32x4){c_, c_, c_, c_}; \
    } while (0)
    {
        u32x4 tk0, tk1, tv0, tv1, uk0, uk1, uv0, uv1;
        KV_LOAD(0);
        tk0 = sk0; tk1 = sk1; tv0 = sv0; tv1 = sv1;
        KV_LOAD(1);
        uk0 = sk0; uk1 = sk1; uv0 = sv0; uv1 = sv1;
        KV_LOAD(2);
        SBAR();
        if (threadIdx.x < 128) KV_CONST(threadIdx.x >> 6);
        accq = MFMA(wext(acc_cs[0], h), xext(__uint_as_float(stq[0]), h), accq);
        const float qs = __uint_as_float(stq[1]) * (0.25f * 1.4426950408889634f);
        qa = BC8(pack8(accq, 0, qs));
        qb = BC8(pack8(accq, 8, qs));
        SBAR();
        *(LAS u32x4*)(LW + kwr) = tk0; *(LAS u32x4*)(LW + kwr + 1024) = tk1;
        *(LAS u32x4*)(LW + vwr) = tv0; *(LAS u32x4*)(LW + vwr + VT) = tv1;
        *(LAS u32x4*)(LW + A_SLOT + kwr) = uk0; *(LAS u32x4*)(LW + A_SLOT + kwr + 1024) = uk1;
        *(LAS u32x4*)(LW + A_SLOT + vwr) = uv0; *(LAS u32x4*)(LW + A_SLOT + vwr + VT) = uv1;
    }
    asm volatile("s_waitcnt lgkmcnt(0)\n\ts_barrier" ::: "memory");
    if (w == 2) KV_CONST(2);
    const int kln = lo16, vln = 8192 + h * VH + (r < 17 ? r : 17) * 16;
    f32x16 nma, nmb;
    bf16x8 kf = lds_b128(L + kln);
    bf16x8 v0 = lds_b128(L + vln), v1 = lds_b128(L + vln + 2 * VH);
    {
        f32x16 z = {};
        f32x16 s0 = MFMA(kf, qa, z), s1 = MFMA(kf, qb, z);
        const float ma = swap_max(max16(s0)), mb = swap_max(max16(s1));
#pragma unroll
        for (int i = 0; i < 16; ++i) { nma[i] = -ma; nmb[i] = -mb; }
    }
    f32x16 oa = {}, ob = {};
    f32x16 sa = MFMA(kf, qa, nma), sb;
    float e0, e1;
    u32x4 pp = {0u, 0u, 0u, 0u};
    bf16x8 vq = v1;
    e0 = EXP2(sa[0]); e1 = EXP2(sa[1]);
#define ATT_HALF(SX, OX, OP, SY, NEXT, V0, V1, LOADS)                                                   \
    do {                                                                                                \
        OP = MFMA(vq, BC8(pp), OP); SBAR();                                                             \
        float e2 = EXP2(SX[2]), e3 = EXP2(SX[3]); LOADS; float e4 = EXP2(SX[4]), e5 = EXP2(SX[5]); SBAR(); \
        SY = NEXT; SBAR();                                                                              \
        float e6 = EXP2(SX[6]), e7 = EXP2(SX[7]);                                                       \
        u32x4 p0; p0[0] = cvtpk(e0, e1); p0[1] = cvtpk(e2, e3); p0[2] = cvtpk(e4, e5); p0[3] = cvtpk(e6, e7); SBAR(); \
        e0 = EXP2(SX[8]); e1 = EXP2(SX[9]); SBAR();                                                     \
        OX = MFMA(V0, BC8(p0), OX); SBAR();                                                             \
        e2 = EXP2(SX[10]); e3 = EXP2(SX[11]); e4 = EXP2(SX[12]); e5 = EXP2(SX[13]); e6 = EXP2(SX[14]); e7 = EXP2(SX[15]); \
        pp[0] = cvtpk(e0, e1); pp[1] = cvtpk(e2, e3); pp[2] = cvtpk(e4, e5); pp[3] = cvtpk(e6, e7); vq = V1; SBAR(); \
        e0 = EXP2(SY[0]); e1 = EXP2(SY[1]); SBAR();                                                     \
    } while (0)
#define ATT_TILE(KN, VN, EXTRA)                                                                         \
    do {                                                                                                \
        bf16x8 kn, n0, n1;                                                                              \
        ATT_HALF(sa, oa, ob, sb, MFMA(kf, qb, nmb), v0, v1, kn = lds_b128(KN); EXTRA);                  \
        ATT_HALF(sb, ob, oa, sa, MFMA(kn, qa, nma), v0, v1, (n0 = lds_b128(VN), n1 = lds_b128((VN) + 2 * VH))); \
        kf = kn; v0 = n0; v1 = n1;                                                                      \
    } while (0)
    __builtin_amdgcn_s_setprio(1);
    int cur = 0, nxt = A_SLOT, fre = 2 * A_SLOT;
#pragma unroll 1
    for (int c = 0; c < 8; ++c) {
        LAS const char* kc = L + cur + kln;
        LAS const char* vc = L + cur + vln;
        u64 fv = tag;
        if (rdy != ~0ull) fv = __hip_atomic_load(kvf, __ATOMIC_RELAXED, __HIP_MEMORY_SCOPE_AGENT);
        SBAR();
        ATT_TILE(kc + 1 * 1024, vc + 1 * VT, (void)0);
        ATT_TILE(kc + 2 * 1024, vc + 2 * VT, KV_WRITE((LW + fre)));
        if (rdy != ~0ull) {
            rdy = __ballot(fv == tag);
            const unsigned long long need = c < 5 ? 0xFull << (4 * (c + 3)) : 0ull;
            for (unsigned spins = 0; (rdy & need) != need && spins < SPIN_CAP; ++spins)
                rdy = __ballot(__hip_atomic_load(kvf, __ATOMIC_RELAXED, __HIP_MEMORY_SCOPE_AGENT) == tag);
            __builtin_amdgcn_fence(__ATOMIC_ACQUIRE, "wavefront");
            asm volatile("" ::: "memory");
        }
        SBAR();
        ATT_TILE(kc + 3 * 1024, vc + 3 * VT, KV_LOAD(c + 3));
        ATT_TILE(kc + 4 * 1024, vc + 4 * VT, (void)0);
        ATT_TILE(kc + 5 * 1024, vc + 5 * VT, (void)0);
        ATT_TILE(kc + 6 * 1024, vc + 6 * VT, (void)0);
        ATT_TILE(kc + 7 * 1024, vc + 7 * VT, (void)0);
        ATT_TILE(L + nxt + kln, L + nxt + vln, (void)0);
        asm volatile("s_waitcnt lgkmcnt(0)\n\ts_barrier" ::: "memory");
        const int t_ = cur; cur = nxt; nxt = fre; fre = t_;
    }
    ob = MFMA(vq, BC8(pp), ob);
#undef ATT_TILE
#undef ATT_HALF
#undef KV_LOAD
#undef KV_WRITE
#undef KV_CONST
    float la, lb;
    {
        la = swap_add(oa[8]); lb = swap_add(ob[8]);
        const bool bad = !(la > 9.094947e-13f && la < 1.0995116e12f && lb > 9.094947e-13f && lb < 1.0995116e12f);
                if (__builtin_expect(__any(bad), 0)) {
            oa = attn_slow(rkv, qa, lane); ob = attn_slow(rkv, qb, lane);
            la = swap_add(oa[8]); lb = swap_add(ob[8]);
        }
    }
    const __amdgpu_buffer_rsrc_t ro = __builtin_amdgcn_make_buffer_rsrc(
        ws + WS_ACT + ((size_t)(2 * it + rgl) * NKS + 4 * g + 2 * p) * 1024, 0, 2048, 0x00020000);
    __builtin_amdgcn_raw_buffer_store_b128(pack8(oa, 0, __builtin_amdgcn_rcpf(la)), ro, lo16, 0, SC1);
    __builtin_amdgcn_raw_buffer_store_b128(pack8(ob, 0, __builtin_amdgcn_rcpf(lb)), ro, lo16 + 1024, 0, SC1);
    asm volatile("s_waitcnt vmcnt(0)\n\ts_barrier" ::: "memory");
    if (threadIdx.x == 0) set_flag(ws, SY_O + (size_t)(it * 8 + g) * FL, tag);
}

constexpr int J_X = 49152, J_W = 16384;
DI void kv_issue(const u16* __restrict__ x, const u16* __restrict__ wkv, char* smem, const int job, const int w, const int lane) {
    const unsigned lds0 = (unsigned)(uintptr_t)smem;
    const int prow = lane >> 3, pslot = lane & 7;
    const unsigned joA = (unsigned)(prow * DM * 2 + ((pslot ^ (prow >> 1)) * 16));
    const unsigned joB = (unsigned)((8 + prow) * DM * 2 + ((pslot ^ (4 + (prow >> 1))) * 16));
    const char* xj = (const char*)(x + (size_t)(job >> 1) * 32 * DM) + w * 256;
    const char* kvw = (const char*)wkv + w * 4096;
#pragma unroll
    for (int sl = 0; sl < 2; ++sl) {
#pragma unroll
        for (int i = 0; i < 4; ++i)
            glds16s(xj + sl * 128 + (i >> 1) * (16 * DM * 2), (i & 1) ? joB : joA, lds0 + J_X + w * 8192 + sl * 4096 + i * 1024);
        glds16s(kvw + sl * 2048, lane * 16, lds0 + J_W + w * 4096 + sl * 2048);
        glds16s(kvw + sl * 2048 + 1024, lane * 16, lds0 + J_W + w * 4096 + sl * 2048 + 1024);
    }
}
template <int YOUNG>
DI void kv_job(char* __restrict__ ws, char* smem, const int job, const int w, const int lane) {
    const int r = lane & 31, h = lane >> 5, q4 = (lane & 15) >> 2, p4 = lane & 3;
    const int rg = job >> 1;
    const bool isv = job & 1;
    const unsigned lo16 = lane * 16;
    LAS const char* L = (LAS const char*)smem;
    const bf16x8 sel = make_sel(lane);
    LAS const char* xl = L + J_X + w * 8192 + r * 128;
    LAS const char* kl = L + J_W + w * 4096 + (8 * h + q4) * 32 + p4 * 8;
    const int fj = (r >> 1) & 7;
    f32x16 acck = {}, accx = {};
    f32x4 acc_ck = {}, acc_rs = {};
    bf16x8 xf[8], kf[8];
    asm volatile("s_waitcnt vmcnt(%0)" ::"n"(6 + YOUNG) : "memory");
#pragma unroll
    for (int k = 0; k < 4; ++k) { xf[k] = lds_b128(xl + (((2 * k + h) ^ fj) * 16)); kf[k] = lds_tr2<128>(kl + k * 512); }
    asm volatile("s_waitcnt vmcnt(%0)" ::"n"(YOUNG) : "memory");
#pragma unroll
    for (int k = 4; k < 8; ++k) { xf[k] = lds_b128(xl + 4096 + (((2 * (k - 4) + h) ^ fj) * 16)); kf[k] = lds_tr2<128>(kl + k * 512); }
    if (isv) {
#pragma unroll
        for (int k = 0; k < 8; ++k) acck = MFMA(xf[k], kf[k], acck);
    } else {
#pragma unroll
        for (int k = 0; k < 8; ++k) acck = MFMA(kf[k], xf[k], acck);
    }
#pragma unroll
    for (int k = 0; k < 8; ++k) {
        accx = MFMA(xf[k], xf[k], accx);
        acc_rs = MFMA16(sel, xf[k], acc_rs);
        acc_ck = MFMA16(sel, kf[k], acc_ck);
    }
    char* pq = smem + J_W + w * 4096;
    if (isv) {
        if (r < 16) {
#pragma unroll
            for (int q = 0; q < 4; ++q)
                *(f32x4*)(pq + q * 512 + (h * 16 + r) * 16) = (f32x4){acck[4 * q], acck[4 * q + 1], acck[4 * q + 2], acck[4 * q + 3]};
        }
    } else {
        *(f32x4*)(pq + lo16) = (f32x4){acck[0], acck[1], acck[2], acck[3]};
        *(f32x4*)(pq + 1024 + lo16) = (f32x4){acck[4], acck[5], acck[6], acck[7]};
    }
    const float dg = diag32(accx, r);
    if (h == ((r >> 2) & 1)) { ((float*)(pq + 2048))[r] = acc_rs[0]; ((float*)(pq + 2176))[r] = dg; }
    if (lane < 16) ((float*)(pq + 2304))[lane] = acc_ck[0];
    asm volatile("s_waitcnt lgkmcnt(0)\n\ts_barrier" ::: "memory");
    if (w == 0) {
        const char* p0 = smem + J_W;
        float sum = 0.f, sq = 0.f, ck = 0.f;
#pragma unroll
        for (int v = 0; v < 4; ++v) {
            sum += ((const float*)(p0 + v * 4096 + 2048))[r];
            sq += ((const float*)(p0 + v * 4096 + 2176))[r];
            ck += ((const float*)(p0 + v * 4096 + 2304))[lane & 15];
        }
        float mu, rs;
        ln_stat(sum, sq, mu, rs);
        const bf16x8 ex = xext(mu, h), ek = wext(ck, h);
        const __amdgpu_buffer_rsrc_t rkv_st = __builtin_amdgcn_make_buffer_rsrc(ws, 0, 131072 + 16384, 0x00020000);
        if (isv) {
            float* srs = (float*)(smem + J_W + 2432);
            if (h == 0) srs[r] = rs;
#pragma unroll
            for (int v = 1; v < 4; ++v)
#pragma unroll
                for (int q = 0; q < 4; ++q) {
                    const f32x4 o = *(const f32x4*)(p0 + v * 4096 + q * 512 + (h * 16 + (r & 15)) * 16);
                    acck[4 * q] += o[0]; acck[4 * q + 1] += o[1]; acck[4 * q + 2] += o[2]; acck[4 * q + 3] += o[3];
                }
            acck = MFMA(ex, ek, acck);
            f32x16 v;
#pragma unroll
            for (int q = 0; q < 4; ++q) {
                const f32x4 rr = *(const f32x4*)(srs + 8 * q + 4 * h);
#pragma unroll
                for (int i = 0; i < 4; ++i) v[4 * q + i] = acck[4 * q + i] * rr[i];
            }
            const unsigned vo = r < 16 ? (unsigned)WS_VF + h * 256 + r * 16 : 0x40000000u;
            __builtin_amdgcn_raw_buffer_store_b128(pack8(v, 0, 1.0f), rkv_st, vo + rg * 1024, 0, SC1);
            __builtin_amdgcn_raw_buffer_store_b128(pack8(v, 8, 1.0f), rkv_st, vo + rg * 1024 + 512, 0, SC1);
        } else {
#pragma unroll
            for (int v = 1; v < 4; ++v)
#pragma unroll
                for (int q = 0; q < 2; ++q) {
                    const f32x4 o = *(const f32x4*)(p0 + v * 4096 + q * 1024 + lo16);
                    acck[4 * q] += o[0]; acck[4 * q + 1] += o[1]; acck[4 * q + 2] += o[2]; acck[4 * q + 3] += o[3];
                }
            acck = MFMA(ek, ex, acck);
            u32x2 ms = {__float_as_uint(mu), __float_as_uint(rs)};
            __builtin_amdgcn_raw_buffer_store_b64(ms, rkv_st, h == 0 ? (unsigned)WS_STAT + rg * 256 + r * 8 : 0x40000000u, 0, SC1);
            __builtin_amdgcn_raw_buffer_store_b128(pack8(acck, 0, rs), rkv_st, lo16 + rg * 1024, 0, SC1);
        }
    }
}

constexpr int G_W = 32768;
DI void g_gateup(const u16* __restrict__ x, const u16* __restrict__ wg, const u16* __restrict__ wu, char* __restrict__ ws,
                 char* smem, const int bid, const int rt, const int cg, const int job, const int w, const int lane, u64& tag) {
    const int wr = w & 1, wn = w >> 1;
    const int r = lane & 31, h = lane >> 5;
    const unsigned lds0 = (unsigned)(uintptr_t)smem;
    const unsigned lo16 = lane * 16;
    LAS const char* L = (LAS const char*)smem;
    const int prow = lane >> 3, pslot = lane & 7;
    const unsigned xoA = (unsigned)((32 * w + prow) * DM * 2 + ((pslot ^ (prow >> 1)) * 16));
    const unsigned xoB = (unsigned)((32 * w + 8 + prow) * DM * 2 + ((pslot ^ (4 + (prow >> 1))) * 16));
    const unsigned wo0 = (unsigned)((8 * w + prow) * DF * 2 + ((pslot ^ (((prow >> 1) & 1) << 2)) * 16));
    const unsigned wo1 = wo0 + 128;
    const char* xb = (const char*)(x + (size_t)rt * 128 * DM);
    const char* wA = (const char*)(wg + cg * 128);
    const char* wB = (const char*)(wu + cg * 128);
#define G_DMAX(j, i)                                                                                      \
    do {                                                                                                  \
        if ((j) < 8) glds16s(xb + (j) * 128 + ((i) >> 1) * (16 * DM * 2), ((i) & 1) ? xoB : xoA,          \
                             lds0 + ((j) & 1) * 16384 + w * 4096 + (i) * 1024);                           \
    } while (0)
#define G_DMAW(u, i)                                                                                      \
    do {                                                                                                  \
        if ((u) < 16) glds16s((((i) >> 1) ? wB : wA) + (size_t)(u) * (32 * DF * 2), ((i) & 1) ? wo1 : wo0, \
                              lds0 + G_W + ((u) % 3) * 16384 + w * 4096 + (i) * 1024);                    \
    } while (0)
#pragma unroll
    for (int i = 0; i < 4; ++i) G_DMAX(0, i);
#pragma unroll
    for (int i = 0; i < 4; ++i) G_DMAW(0, i);
    tag = load_epoch(ws) + 1;
    if (job >= 0) kv_job<8>(ws, smem, job, w, lane);
#pragma unroll
    for (int i = 0; i < 4; ++i) G_DMAW(1, i);

    const int ga = 2 * wr + wn, gb = 2 * wr + 1 - wn;
    const int fx = (r >> 1) & 7;
    const int blk = (lane >> 4) & 1, q4 = (lane & 15) >> 2, p4 = lane & 3;
    const int wl0 = G_W + h * 4096 + wn * 1024 + q4 * 128 + (((blk * 2 + (p4 >> 1)) ^ (((q4 >> 1) & 1) << 2)) * 16) + (p4 & 1) * 8;
    const int wl1 = wl0 ^ 64;
    const int c0 = wl0 + wr * 2048, c1 = wl1 + wr * 2048, d0 = wl0 + (1 - wr) * 2048, d1 = wl1 + (1 - wr) * 2048;
    f32x16 ac0a = {}, ac0b = {}, ac1a = {}, ac1b = {}, ad0a = {}, ad0b = {}, ad1a = {}, ad1b = {};
    bf16x8 xa[2], xbf[2], fc0[2], fc1[2], fd0[2], fd1[2];
#define G_XS(u, j) (L + (((u) >> 1) & 1) * 16384 + r * 128 + (((2 * (((u) & 1) * 2 + (j)) + h) ^ fx) * 16))
#define G_WO(u, j) (((u) % 3) * 16384 + (j) * 8192)
#define G_READ(set, u, j)                                                             \
    do {                                                                              \
        fc0[set] = lds_tr2<512>(L + c0 + G_WO(u, j));                                 \
        xa[set] = lds_b128(G_XS(u, j) + ga * 4096);                                   \
        xbf[set] = lds_b128(G_XS(u, j) + gb * 4096);                                  \
        fc1[set] = lds_tr2<512>(L + c1 + G_WO(u, j));                                 \
        fd0[set] = lds_tr2<512>(L + d0 + G_WO(u, j));                                 \
        fd1[set] = lds_tr2<512>(L + d1 + G_WO(u, j));                                 \
        SBAR();                                                                       \
    } while (0)
#define G_STEP(set, RD, nu, nj, D0, D1, D2, D3)                                       \
    do {                                                                              \
        ac0a = MFMA(fc0[set], xa[set], ac0a); SBAR();                                 \
        if (RD) { fc0[1 - set] = lds_tr2<512>(L + c0 + G_WO(nu, nj)); SBAR(); }       \
        ac0b = MFMA(fc0[set], xbf[set], ac0b); SBAR();                                \
        if (RD) { xa[1 - set] = lds_b128(G_XS(nu, nj) + ga * 4096); xbf[1 - set] = lds_b128(G_XS(nu, nj) + gb * 4096); SBAR(); } \
        ac1a = MFMA(fc1[set], xa[set], ac1a); SBAR();                                 \
        if (RD) { fc1[1 - set] = lds_tr2<512>(L + c1 + G_WO(nu, nj)); SBAR(); }       \
        ac1b = MFMA(fc1[set], xbf[set], ac1b); SBAR();                                \
        if (RD) { fd0[1 - set] = lds_tr2<512>(L + d0 + G_WO(nu, nj)); SBAR(); }       \
        ad0a = MFMA(fd0[set], xa[set], ad0a); SBAR();                                 \
        if (RD) { fd1[1 - set] = lds_tr2<512>(L + d1 + G_WO(nu, nj)); SBAR(); }       \
        D2;                                                                           \
        ad0b = MFMA(fd0[set], xbf[set], ad0b); SBAR();                                \
        D0;                                                                           \
        ad1a = MFMA(fd1[set], xa[set], ad1a); SBAR();                                 \
        D1;                                                                           \
        ad1b = MFMA(fd1[set], xbf[set], ad1b); SBAR();                                \
        D3;                                                                           \
    } while (0)
    wait_bar<4>();
    if (job >= 0 && threadIdx.x == 0) set_flag(ws, SY_KV + (size_t)job * FL, tag);
    __builtin_amdgcn_s_setprio(0);
    if (lane == 0 && w == 1) set_flag(ws, SY_SEEN + (size_t)(256 + bid) * 8, tag);
    G_READ(0, 0, 0);
    G_DMAW(2, 0); G_DMAW(2, 1); G_DMAX(1, 0); G_DMAX(1, 1);
#pragma unroll
    for (int u = 0; u < 16; ++u) {
        if ((u & 1) == 0) G_STEP(0, true, u, 1, G_DMAW(u + 2, 2), G_DMAW(u + 2, 3), G_DMAX(u / 2 + 1, 2), G_DMAX(u / 2 + 1, 3));
        else              G_STEP(0, true, u, 1, G_DMAW(u + 2, 2), G_DMAW(u + 2, 3), (void)0, (void)0);
        if (u < 15) {
            const int nfly = (u + 2 < 16 ? 4 : 0) + ((((u & 1) == 0) && (u / 2 + 1 < 8)) ? 4 : 0);
            if (nfly == 8) wait_bar<8>(); else if (nfly == 4) wait_bar<4>(); else wait_bar<0>();
        }
        if (u & 1) G_STEP(1, u < 15, u + 1, 0, G_DMAW(u + 3, 0), G_DMAW(u + 3, 1), G_DMAX((u + 1) / 2 + 1, 0), G_DMAX((u + 1) / 2 + 1, 1));
        else       G_STEP(1, true, u + 1, 0, G_DMAW(u + 3, 0), G_DMAW(u + 3, 1), (void)0, (void)0);
    }
#undef G_STEP
#undef G_XS
#undef G_WO
#undef G_READ
#undef G_DMAX
#undef G_DMAW
    wait_flags(ws, (lane & 2) ? (unsigned)(SY_C + (cg + 14 * (lane & 1)) * FL) : (unsigned)(SY_KV + (rt * 4 + 2 * wr + (lane & 1)) * 2 * FL), tag);
    const __amdgpu_buffer_rsrc_t rst = __builtin_amdgcn_make_buffer_rsrc(ws + WS_STAT, 0, 32768, 0x00020000);
    const u32x2 sta = __builtin_amdgcn_raw_buffer_load_b64(rst, rt * 1024 + ga * 256 + r * 8, 0, SC1);
    const u32x2 stb = __builtin_amdgcn_raw_buffer_load_b64(rst, rt * 1024 + gb * 256 + r * 8, 0, SC1);
    const unsigned co_c = 16384 + ((wr * 14 + cg) * 128 + wn * 64 + r) * 4, co_d = 16384 + (((1 - wr) * 14 + cg) * 128 + wn * 64 + r) * 4;
    const float csc0 = __uint_as_float(__builtin_amdgcn_raw_buffer_load_b32(rst, co_c, 0, SC1));
    const float csc1 = __uint_as_float(__builtin_amdgcn_raw_buffer_load_b32(rst, co_c + 128, 0, SC1));
    const float csd0 = __uint_as_float(__builtin_amdgcn_raw_buffer_load_b32(rst, co_d, 0, SC1));
    const float csd1 = __uint_as_float(__builtin_amdgcn_raw_buffer_load_b32(rst, co_d + 128, 0, SC1));
    const float mu_a = __uint_as_float(sta[0]), rs_a = __uint_as_float(sta[1]), mu_b = __uint_as_float(stb[0]), rs_b = __uint_as_float(stb[1]);
    {
        const bf16x8 ea = xext(mu_a, h), eb = xext(mu_b, h);
        const bf16x8 ec0 = wext(csc0, h), ec1 = wext(csc1, h), ed0 = wext(csd0, h), ed1 = wext(csd1, h);
        ac0a = MFMA(ec0, ea, ac0a); ad0a = MFMA(ed0, ea, ad0a);
        ac0b = MFMA(ec0, eb, ac0b); ad0b = MFMA(ed0, eb, ad0b);
        ac1a = MFMA(ec1, ea, ac1a); ad1a = MFMA(ed1, ea, ad1a);
        ac1b = MFMA(ec1, eb, ac1b); ad1b = MFMA(ed1, eb, ad1b);
    }
    const __amdgpu_buffer_rsrc_t rh = __builtin_amdgcn_make_buffer_rsrc(
        ws + WS_ACT + ((size_t)rt * 4 * NKS + 32 + 8 * cg + 4 * wn) * 1024, 0, 4 * NKS * 1024, 0x00020000);
    const float K1c = 2.0f * 0.7978845608028654f * 1.4426950408889634f, K3c = K1c * 0.044715f;
    auto geglu = [&](const f32x16& G, const f32x16& U, float rs, int so) {
        typedef float f32x2 __attribute__((ext_vector_type(2)));
        const float c1_ = -K1c * rs, c3_ = -K3c * rs * rs * rs, ri2 = __builtin_amdgcn_rcpf(rs * rs);
        const f32x2 c1v = {c1_, c1_}, c3v = {c3_, c3_}, riv = {ri2, ri2};
        u32x4 o0, o1;
#pragma unroll
        for (int i = 0; i < 8; ++i) {
            const f32x2 gg = {G[2 * i], G[2 * i + 1]}, uu = {U[2 * i], U[2 * i + 1]};
            const f32x2 t = gg * __builtin_elementwise_fma(gg * gg, c3v, c1v);
            const f32x2 e = {EXP2(t[0]), EXP2(t[1])};
            const f32x2 d = __builtin_elementwise_fma(e, riv, riv);
            const f32x2 rr = {__builtin_amdgcn_rcpf(d[0]), __builtin_amdgcn_rcpf(d[1])};
            const f32x2 hv = (gg * uu) * rr;
            const unsigned pk = cvtpk(hv[0], hv[1]);
            if (i < 4) o0[i] = pk; else o1[i - 4] = pk;
        }
        __builtin_amdgcn_raw_buffer_store_b128(o0, rh, lo16 + so, 0, SC1);
        __builtin_amdgcn_raw_buffer_store_b128(o1, rh, lo16 + so + 1024, 0, SC1);
    };
    const int soa = ga * NKS * 1024, sob = gb * NKS * 1024;
    const int nlow = job >= 0 ? 2 : 3;
#define GE_PRIO(i) do { if (nlow == (i)) __builtin_amdgcn_s_setprio(2); } while (0)
#define GE_HALF()                                                                                        \
    do {                                                                                                 \
        asm volatile("s_waitcnt vmcnt(2)\n\ts_barrier" ::: "memory");     \
        if (threadIdx.x == 0) set_flag(ws, SY_H + (size_t)(rt * 16 + cg) * FL, tag);                     \
    } while (0)
    if (wr == 0) {
        GE_PRIO(0); geglu(ac0a, ad0a, rs_a, soa);        GE_PRIO(1); geglu(ac0b, ad0b, rs_b, sob);
        GE_PRIO(2); geglu(ac1a, ad1a, rs_a, soa + 2048); GE_HALF(); GE_PRIO(3); geglu(ac1b, ad1b, rs_b, sob + 2048);
    } else {
        GE_PRIO(0); geglu(ad0a, ac0a, rs_a, soa);        GE_PRIO(1); geglu(ad0b, ac0b, rs_b, sob);
        GE_PRIO(2); geglu(ad1a, ac1a, rs_a, soa + 2048); GE_HALF(); GE_PRIO(3); geglu(ad1b, ac1b, rs_b, sob + 2048);
    }
#undef GE_HALF
#undef GE_PRIO
    asm volatile("s_waitcnt vmcnt(0) lgkmcnt(0)\n\ts_barrier" ::: "memory");
    if (threadIdx.x == 0) set_flag(ws, SY_H + (size_t)(rt * 16 + cg) * FL + 64, tag);
}

DI void g_retile(const u16* __restrict__ wo, const u16* __restrict__ wd, const u16* __restrict__ wg, const u16* __restrict__ wu,
                 char* __restrict__ ws, char* smem, const int bid,
                 const int j, const int job, const int w, const int lane, u64& tag) {
    const int r = lane & 31, h = lane >> 5;
    const int ng = j >> 1, ks0 = 72 * (j & 1) + 18 * w;
    const __amdgpu_buffer_rsrc_t rw =
        __builtin_amdgcn_make_buffer_rsrc(ws + WS_W2F + ((size_t)ng * NKS + ks0) * 1024, 0, 18 * 1024, 0x00020000);
    tag = load_epoch(ws) + 1;
    if (job >= 0) kv_job<0>(ws, smem, job, w, lane);
    if (job >= 0 && w == 0) {
        asm volatile("s_waitcnt vmcnt(0)" ::: "memory");
        if (lane == 0) set_flag(ws, SY_KV + (size_t)job * FL, tag);
    }
    if (lane == 0 && w == 1) set_flag(ws, SY_SEEN + (size_t)(256 + bid) * 8, tag);
    if (j < 28) {
        const int t = threadIdx.x;
        const char* src = (const char*)((j < 14 ? wg : wu) + (j < 14 ? j : j - 14) * 128) + (size_t)(t >> 4) * 32 * (DF * 2) + (t & 15) * 16;
        float cs[8] = {0.f, 0.f, 0.f, 0.f, 0.f, 0.f, 0.f, 0.f};
#pragma unroll 1
        for (int b = 0; b < 2; ++b) {
            u32x4 v[16];
#pragma unroll
            for (int i = 0; i < 16; ++i) v[i] = *(const u32x4*)(src + (size_t)(b * 16 + i) * (DF * 2));
#pragma unroll
            for (int i = 0; i < 16; ++i)
#pragma unroll
                for (int d = 0; d < 4; ++d) {
                    const unsigned u = v[i][d];
                    cs[2 * d] += __uint_as_float(u << 16);
                    cs[2 * d + 1] += __uint_as_float(u & 0xffff0000u);
                }
        }
        *(f32x4*)(smem + t * 32) = (f32x4){cs[0], cs[1], cs[2], cs[3]};
        *(f32x4*)(smem + t * 32 + 16) = (f32x4){cs[4], cs[5], cs[6], cs[7]};
        __syncthreads();
        if (w == 0) {
            float s0 = 0.f, s1 = 0.f;
#pragma unroll
            for (int k = 0; k < 16; ++k) { s0 += ((const float*)smem)[k * 128 + 2 * lane]; s1 += ((const float*)smem)[k * 128 + 2 * lane + 1]; }
            const __amdgpu_buffer_rsrc_t rc = __builtin_amdgcn_make_buffer_rsrc(ws + WS_CS + j * 512, 0, 512, 0x00020000);
            __builtin_amdgcn_raw_buffer_store_b64((u32x2){__float_as_uint(s0), __float_as_uint(s1)}, rc, lane * 8, 0, SC1);
            asm volatile("s_waitcnt vmcnt(0)" ::: "memory");
            if (lane == 0) set_flag(ws, SY_C + (size_t)j * FL, tag);
        }
    }
#pragma unroll 1
    for (int b = 0; b < 3; ++b) {
        u16 v[6][8];
#pragma unroll
        for (int f = 0; f < 6; ++f) {
            const int ks = ks0 + b * 6 + f;
            const u16* src = (ks < 32 ? wo + (size_t)ks * 16 * DM : wd + (size_t)(ks - 32) * 16 * DM) + ng * 32 + r + h * 4 * DM;
#pragma unroll
            for (int e = 0; e < 8; ++e) v[f][e] = src[(8 * (e >> 2) + (e & 3)) * DM];
        }
#pragma unroll
        for (int f = 0; f < 6; ++f) {
            u32x4 o;
#pragma unroll
            for (int i = 0; i < 4; ++i) o[i] = (unsigned)v[f][2 * i] | ((unsigned)v[f][2 * i + 1] << 16);
            __builtin_amdgcn_raw_buffer_store_b128(o, rw, lane * 16 + (b * 6 + f) * 1024, 0, SC1);
        }
    }
    asm volatile("s_waitcnt vmcnt(0)\n\ts_barrier" ::: "memory");
    if (threadIdx.x == 0) set_flag(ws, SY_W + (size_t)j * FL, tag);
    if (j == 0 && w == 0) {
#pragma unroll 1
        for (int i = 0; i < 8; ++i) wait_flags(ws, SY_SEEN + i * 512 + lane * 8, tag);
        if (lane == 0) set_flag(ws, SY_EPOCH, tag);
    }
}

DI void role_g(const u16* __restrict__ x, const u16* __restrict__ wk, const u16* __restrict__ wv, const u16* __restrict__ wo, const u16* __restrict__ wg, const u16* __restrict__ wu,
               const u16* __restrict__ wd, char* __restrict__ ws, u16* __restrict__ out, char* smem, const int bid, const int w,
               const int lane) {
    const int xcd = bid & 7, idx = bid >> 3;
    const int q_ = xcd >> 1, l_ = idx >> 3;
    const int rt = (xcd & 1) * 8 + (idx & 7), cg = q_ < 2 ? 4 * q_ + l_ : (l_ < 3 ? 2 + 3 * q_ + l_ : 14), lj = (xcd - 4) * 8 + (idx & 7);
    const int it = xcd * 4 + (idx & 3), g = idx >> 2;
    const unsigned lo16 = lane * 16;
    u64 tag;
    __builtin_amdgcn_s_setprio(3);
    const int job = idx >= 16 ? xcd * 16 + (idx - 16) : -1;
    if (job >= 0) kv_issue(x, (job & 1) ? wv : wk, smem, job, w, lane);
    if (cg < 14) g_gateup(x, wg, wu, ws, smem, bid, rt, cg, job, w, lane, tag);
    else g_retile(wo, wd, wg, wu, ws, smem, bid, lj, job, w, lane, tag);
    __builtin_amdgcn_s_setprio(0);

    const unsigned hfo = (unsigned)(SY_H + ((it >> 1) * 16 + ((7 * w) >> 1) + (lane & 3)) * FL);
    wait_flags(ws, lane < 8 ? hfo : (unsigned)(SY_W + (4 * g + (lane & 3)) * FL), tag);
    const __amdgpu_buffer_rsrc_t ra =
        __builtin_amdgcn_make_buffer_rsrc(ws + WS_ACT + (size_t)(2 * it) * NKS * 1024, 0, 2 * NKS * 1024, 0x00020000);
    const __amdgpu_buffer_rsrc_t rw =
        __builtin_amdgcn_make_buffer_rsrc(ws + WS_W2F + (size_t)(2 * g) * NKS * 1024, 0, 2 * NKS * 1024, 0x00020000);
    const int so = (32 + 28 * w) * 1024;
    constexpr int PD = 6;
    f32x16 acc00 = {}, acc01 = {}, acc10 = {}, acc11 = {};
    u32x4 fw0[8], fw1[8], fa0[8], fa1[8];
#define G_LOAD(i, k)                                                                                   \
    do {                                                                                               \
        fw0[i] = __builtin_amdgcn_raw_buffer_load_b128(rw, lo16, so + (k) * 1024, SC1);                \
        fa0[i] = __builtin_amdgcn_raw_buffer_load_b128(ra, lo16, so + (k) * 1024, SC1);                \
        fa1[i] = __builtin_amdgcn_raw_buffer_load_b128(ra, lo16, so + (NKS + (k)) * 1024, SC1);        \
        fw1[i] = __builtin_amdgcn_raw_buffer_load_b128(rw, lo16, so + (NKS + (k)) * 1024, SC1);        \
        SBAR();                                                                                        \
    } while (0)
#define G_MMA4(i)                                                                                      \
    do {                                                                                               \
        acc00 = MFMA(BC8(fw0[i]), BC8(fa0[i]), acc00); acc01 = MFMA(BC8(fw0[i]), BC8(fa1[i]), acc01);  \
        acc10 = MFMA(BC8(fw1[i]), BC8(fa0[i]), acc10); acc11 = MFMA(BC8(fw1[i]), BC8(fa1[i]), acc11);  \
        SBAR();                                                                                        \
    } while (0)
#define KORD(n) ((n) < 14 ? ((n) >> 1) * 4 + ((n) & 1) : (((n) - 14) >> 1) * 4 + 2 + ((n) & 1))
#pragma unroll
    for (int i = 0; i < PD; ++i) G_LOAD(i, KORD(i));
#pragma unroll
    for (int k = 0; k < 28; ++k) {
        G_MMA4(k % PD);
        if (k + PD == 14) wait_flags(ws, hfo + 64, tag);
        if (k + PD < 28) G_LOAD(k % PD, KORD(k + PD));
    }
#undef KORD
#pragma unroll
    for (int i = 0; i < 8; ++i) {
        fw0[i] = __builtin_amdgcn_raw_buffer_load_b128(rw, lo16, (8 * w + i) * 1024, SC1);
        fw1[i] = __builtin_amdgcn_raw_buffer_load_b128(rw, lo16, (NKS + 8 * w + i) * 1024, SC1);
    }
    u32x4 xv[2];
#pragma unroll
    for (int u = 0; u < 2; ++u) {
        const int t2 = threadIdx.x + 256 * u;
        xv[u] = *(const u32x4*)(x + (size_t)(it * 64 + (t2 >> 3)) * DM + g * 64 + (t2 & 7) * 8);
    }
    wait_flags(ws, (unsigned)(SY_O + (it * 8 + 2 * w + (lane & 1)) * FL), tag);
    __builtin_amdgcn_s_setprio(2);
#pragma unroll
    for (int i = 0; i < 8; ++i) {
        fa0[i] = __builtin_amdgcn_raw_buffer_load_b128(ra, lo16, (8 * w + i) * 1024, SC1);
        fa1[i] = __builtin_amdgcn_raw_buffer_load_b128(ra, lo16, (NKS + 8 * w + i) * 1024, SC1);
    }
    SBAR();
#pragma unroll
    for (int i = 0; i < 8; ++i) G_MMA4(i);
#undef G_LOAD
#undef G_MMA4
    char* dst = smem + w * 16640 + lo16;
#pragma unroll
    for (int q = 0; q < 4; ++q) {
        *(f32x4*)(dst + (0 + q) * 1040) = (f32x4){acc00[4 * q], acc00[4 * q + 1], acc00[4 * q + 2], acc00[4 * q + 3]};
        *(f32x4*)(dst + (4 + q) * 1040) = (f32x4){acc01[4 * q], acc01[4 * q + 1], acc01[4 * q + 2], acc01[4 * q + 3]};
        *(f32x4*)(dst + (8 + q) * 1040) = (f32x4){acc10[4 * q], acc10[4 * q + 1], acc10[4 * q + 2], acc10[4 * q + 3]};
        *(f32x4*)(dst + (12 + q) * 1040) = (f32x4){acc11[4 * q], acc11[4 * q + 1], acc11[4 * q + 2], acc11[4 * q + 3]};
    }
    const __amdgpu_buffer_rsrc_t rout = __builtin_amdgcn_make_buffer_rsrc(out + (size_t)it * 64 * DM + g * 64, 0, 63 * DM * 2 + 128, 0x00020000);
    __syncthreads();
#pragma unroll
    for (int u = 0; u < 2; ++u) {
        const int t2 = threadIdx.x + 256 * u, row = t2 >> 3, c8 = t2 & 7;
        const int a = c8 >> 2, q = c8 & 3, b = row >> 5, rr = row & 31;
        const char* s0 = smem + ((a * 2 + b) * 4 + q) * 1040 + rr * 16;
        f32x4 lo = {__uint_as_float(xv[u][0] << 16), __uint_as_float(xv[u][0] & 0xffff0000u), __uint_as_float(xv[u][1] << 16), __uint_as_float(xv[u][1] & 0xffff0000u)};
        f32x4 hi = {__uint_as_float(xv[u][2] << 16), __uint_as_float(xv[u][2] & 0xffff0000u), __uint_as_float(xv[u][3] << 16), __uint_as_float(xv[u][3] & 0xffff0000u)};
#pragma unroll
        for (int gw = 0; gw < 4; ++gw) {
            lo += *(const f32x4*)(s0 + gw * 16640);
            hi += *(const f32x4*)(s0 + gw * 16640 + 512);
        }
        u32x4 o = {cvtpk(lo[0], lo[1]), cvtpk(lo[2], lo[3]), cvtpk(hi[0], hi[1]), cvtpk(hi[2], hi[3])};
        __builtin_amdgcn_raw_buffer_store_b128(o, rout, (row * DM + c8 * 8) * 2, 0, SC1);
    }
}

__global__ __launch_bounds__(256, 2) void fused_block(const u16* __restrict__ x, const u16* __restrict__ wq,
                                                      const u16* __restrict__ wk, const u16* __restrict__ wv,
                                                      const u16* __restrict__ wo, const u16* __restrict__ wg,
                                                      const u16* __restrict__ wu, const u16* __restrict__ wd,
                                                      char* __restrict__ ws, u16* __restrict__ out) {
    __shared__ __attribute__((aligned(16))) char smem[81920];
    const int lane = threadIdx.x & 63;
    const int w = __builtin_amdgcn_readfirstlane(threadIdx.x >> 6);
    if (blockIdx.x < 256) role_a(x, wq, ws, smem, blockIdx.x, w, lane);
    else role_g(x, wk, wv, wo, wg, wu, wd, ws, out, smem, blockIdx.x - 256, w, lane);
}

extern "C" void kernel_launch(void* const* d_in, const int* in_sizes, int n_in, void* d_out, int out_size, void* d_ws,
                              size_t ws_size, hipStream_t stream) {
    fused_block<<<512, 256, 0, stream>>>((const u16*)d_in[0], (const u16*)d_in[1], (const u16*)d_in[2], (const u16*)d_in[3],
                                         (const u16*)d_in[4], (const u16*)d_in[6], (const u16*)d_in[7], (const u16*)d_in[8],
                                         (char*)d_ws, (u16*)d_out);
}
```

```cpp
#include <hip/hip_runtime.h>
#include <hip/hip_bf16.h>
#include <stdint.h>

typedef __bf16 bf16x8 __attribute__((ext_vector_type(8)));
typedef __bf16 bf16x2 __attribute__((ext_vector_type(2)));
typedef short s16x4 __attribute__((ext_vector_type(4)));
typedef float f32x16 __attribute__((ext_vector_type(16)));
typedef float f32x4 __attribute__((ext_vector_type(4)));
typedef unsigned u32x4 __attribute__((ext_vector_type(4)));
typedef unsigned u32x2 __attribute__((ext_vector_type(2)));
typedef unsigned short u16;
typedef unsigned long long u64;
typedef __attribute__((address_space(1))) u64 gu64;
#define LAS __attribute__((address_space(3)))
#define DI __device__ __forceinline__
#define MFMA(a, b, c) __builtin_amdgcn_mfma_f32_32x32x16_bf16((a), (b), (c), 0, 0, 0)
#define MFMA16(a, b, c) __builtin_amdgcn_mfma_f32_16x16x32_bf16((a), (b), (c), 0, 0, 0)
#define SBAR() __builtin_amdgcn_sched_barrier(0)
#define EXP2(v) __builtin_amdgcn_exp2f(v)
#define BC8(v) __builtin_bit_cast(bf16x8, v)
#define SC1 16

constexpr int DM = 512, DF = 1792;
constexpr int NKS = 144;
constexpr size_t WS_VF = 65536;
constexpr size_t WS_STAT = 131072;
constexpr size_t WS_CS = 131072 + 16384;
constexpr size_t WS_SYNC = 131072 + 32768;
constexpr size_t SY_EPOCH = 0, SY_SEEN = 4096, SY_W = 8192, SY_C = 12288, SY_KV = 16384, SY_O = 32768, SY_H = 65536;
constexpr int FL = 128;
constexpr size_t WS_ACT = 262144;
constexpr size_t WS_W2F = WS_ACT + (size_t)64 * NKS * 1024;
constexpr unsigned SPIN_CAP = 1u << 18;

DI unsigned cvtpk(float lo, float hi) {
    bf16x2 r = {(__bf16)lo, (__bf16)hi};
    return __builtin_bit_cast(unsigned, r);
}
DI float bf_round(float v) { return (float)(__bf16)v; }
DI float swap_add(float v) {
    auto rr = __builtin_amdgcn_permlane32_swap(__float_as_uint(v), __float_as_uint(v), false, false);
    return __uint_as_float(rr[0]) + __uint_as_float(rr[1]);
}
DI float swap_max(float v) {
    auto rr = __builtin_amdgcn_permlane32_swap(__float_as_uint(v), __float_as_uint(v), false, false);
    return __builtin_fmaxf(__uint_as_float(rr[0]), __uint_as_float(rr[1]));
}
DI void glds16s(const void* sbase, unsigned voff, unsigned lds_base) {
    asm volatile("s_mov_b32 m0, %2\n\ts_nop 0\n\tglobal_load_lds_dwordx4 %0, %1" ::"v"(voff), "s"(sbase), "s"(lds_base) : "memory");
    SBAR();
}
template <int N> DI void wait_bar() { asm volatile("s_waitcnt vmcnt(%0) lgkmcnt(0)\n\ts_barrier" ::"n"(N) : "memory"); }
DI bf16x8 lds_b128(LAS const char* p) { return *(LAS const bf16x8*)p; }
template <int STEP> DI bf16x8 lds_tr2(LAS const char* p) {
    s16x4 lo = __builtin_amdgcn_ds_read_tr16_b64_v4i16((LAS s16x4*)p);
    s16x4 hi = __builtin_amdgcn_ds_read_tr16_b64_v4i16((LAS s16x4*)(p + STEP));
    return __builtin_bit_cast(bf16x8, __builtin_shufflevector(lo, hi, 0, 1, 2, 3, 4, 5, 6, 7));
}
DI u32x4 pack8(const f32x16& v, int o, float s) {
    u32x4 r;
#pragma unroll
    for (int i = 0; i < 4; ++i) r[i] = cvtpk(v[o + 2 * i] * s, v[o + 2 * i + 1] * s);
    return r;
}
DI void ln_stat(float sum, float sq, float& mu, float& rs) {
    const float mean = sum * (1.0f / DM);
    const float var = __builtin_fmaxf(sq * (1.0f / DM) - mean * mean, 0.f);
    const float sd = bf_round(__builtin_amdgcn_sqrtf(bf_round(bf_round(var) + 1e-5f)));
    mu = bf_round(mean); rs = __builtin_amdgcn_rcpf(sd);
}
DI bf16x8 wext(float cs, int h) {
    const float hi = bf_round(cs);
    u32x4 v = {h == 0 ? cvtpk(hi, cs - hi) : 0u, 0u, 0u, 0u};
    return BC8(v);
}
DI bf16x8 xext(float mu, int h) {
    u32x4 v = {h == 0 ? cvtpk(-mu, -mu) : 0u, 0u, 0u, 0u};
    return BC8(v);
}
DI bf16x8 make_sel(int lane) {
    const unsigned o1 = ((lane & 3) == 0 && ((lane >> 2) & 1) == ((lane >> 4) & 1)) ? 0x3F803F80u : 0u;
    return BC8(((u32x4){o1, o1, o1, o1}));
}
DI float diag32(const f32x16& a, int r) {
    typedef float f32x8 __attribute__((ext_vector_type(8)));
    typedef float f32x2 __attribute__((ext_vector_type(2)));
    const f32x8 t8 = (r & 16) ? __builtin_shufflevector(a, a, 8, 9, 10, 11, 12, 13, 14, 15)
                              : __builtin_shufflevector(a, a, 0, 1, 2, 3, 4, 5, 6, 7);
    const f32x4 t4 = (r & 8) ? __builtin_shufflevector(t8, t8, 4, 5, 6, 7) : __builtin_shufflevector(t8, t8, 0, 1, 2, 3);
    const f32x2 t2 = (r & 2) ? __builtin_shufflevector(t4, t4, 2, 3) : __builtin_shufflevector(t4, t4, 0, 1);
    const float t20 = t2[0], t21 = t2[1];
    return (r & 1) ? t21 : t20;
}
DI u64 load_epoch(const char* ws) {
    u64 e;
    asm volatile("s_load_dwordx2 %0, %1, 0x0\n\ts_waitcnt lgkmcnt(0)" : "=s"(e) : "s"(ws + WS_SYNC + SY_EPOCH) : "memory");
    return e;
}
DI void set_flag(char* ws, size_t off, u64 tag) {
    __hip_atomic_store((gu64*)(ws + WS_SYNC + off), tag, __ATOMIC_RELAXED, __HIP_MEMORY_SCOPE_AGENT);
}
DI void wait_flags(char* ws, unsigned off, u64 tag) {
    gu64* p = (gu64*)(ws + WS_SYNC + off);
    for (unsigned spins = 0; spins < SPIN_CAP; ++spins) {
        const u64 v = __hip_atomic_load(p, __ATOMIC_RELAXED, __HIP_MEMORY_SCOPE_AGENT);
        if (__all(v == tag)) break;
        __builtin_amdgcn_s_sleep(2);
    }
    __builtin_amdgcn_fence(__ATOMIC_ACQUIRE, "wavefront");
    asm volatile("" ::: "memory");
}
DI float max16(const f32x16& s) {
    float a = __builtin_fmaxf(s[0], s[1]);
#pragma unroll
    for (int i = 2; i < 16; i += 2) a = __builtin_fmaxf(__builtin_fmaxf(a, s[i]), s[i + 1]);
    return a;
}

DI f32x16 attn_slow(const __amdgpu_buffer_rsrc_t rkv, const bf16x8 q, const int lane) {
    const int r = lane & 31, h = lane >> 5;
    const unsigned vo = (unsigned)WS_VF + h * 256 + (r & 15) * 16;
    float m = -3.0e38f;
    f32x16 o = {};
#pragma unroll 1
    for (int t = 0; t < 64; ++t) {
        const u32x4 kf = __builtin_amdgcn_raw_buffer_load_b128(rkv, lane * 16, t * 1024, SC1);
        u32x4 v0 = __builtin_amdgcn_raw_buffer_load_b128(rkv, vo, t * 1024, SC1);
        u32x4 v1 = __builtin_amdgcn_raw_buffer_load_b128(rkv, vo + 512, t * 1024, SC1);
        if (r >= 16) { const unsigned c_ = r == 16 ? 0x3F803F80u : 0u; v0 = (u32x4){c_, c_, c_, c_}; v1 = v0; }
        f32x16 z = {};
        f32x16 s = MFMA(BC8(kf), q, z);
        const float mn = __builtin_fmaxf(m, swap_max(max16(s)));
        const float al = EXP2(m - mn);
        m = mn;
#pragma unroll
        for (int i = 0; i < 16; ++i) { s[i] -= mn; o[i] *= al; }
        u32x4 p0, p1;
#pragma unroll
        for (int i = 0; i < 4; ++i) {
            p0[i] = cvtpk(EXP2(s[2 * i]), EXP2(s[2 * i + 1]));
            p1[i] = cvtpk(EXP2(s[8 + 2 * i]), EXP2(s[8 + 2 * i + 1]));
        }
        o = MFMA(BC8(v0), BC8(p0), o);
        o = MFMA(BC8(v1), BC8(p1), o);
    }
    return o;
}

constexpr int J_X = 49152, J_W = 16384;
DI void kv_issue(const u16* __restrict__ x, const u16* __restrict__ wkv, char* smem, const int job, const int w, const int lane) {
    const unsigned lds0 = (unsigned)(uintptr_t)smem;
    const int prow = lane >> 3, pslot = lane & 7;
    const unsigned joA = (unsigned)(prow * DM * 2 + ((pslot ^ (prow >> 1)) * 16));
    const unsigned joB = (unsigned)((8 + prow) * DM * 2 + ((pslot ^ (4 + (prow >> 1))) * 16));
    const char* xj = (const char*)(x + (size_t)(job >> 1) * 32 * DM) + w * 256;
    const char* kvw = (const char*)wkv + w * 4096;
#pragma unroll
    for (int sl = 0; sl < 2; ++sl) {
#pragma unroll
        for (int i = 0; i < 4; ++i)
            glds16s(xj + sl * 128 + (i >> 1) * (16 * DM * 2), (i & 1) ? joB : joA, lds0 + J_X + w * 8192 + sl * 4096 + i * 1024);
        glds16s(kvw + sl * 2048, lane * 16, lds0 + J_W + w * 4096 + sl * 2048);
        glds16s(kvw + sl * 2048 + 1024, lane * 16, lds0 + J_W + w * 4096 + sl * 2048 + 1024);
    }
}
template <int YOUNG>
DI void kv_job(char* __restrict__ ws, char* smem, const int job, const int w, const int lane) {
    const int r = lane & 31, h = lane >> 5, q4 = (lane & 15) >> 2, p4 = lane & 3;
    const int rg = job >> 1;
    const bool isv = job & 1;
    const unsigned lo16 = lane * 16;
    LAS const char* L = (LAS const char*)smem;
    const bf16x8 sel = make_sel(lane);
    LAS const char* xl = L + J_X + w * 8192 + r * 128;
    LAS const char* kl = L + J_W + w * 4096 + (8 * h + q4) * 32 + p4 * 8;
    const int fj = (r >> 1) & 7;
    f32x16 acck = {}, accx = {};
    f32x4 acc_ck = {}, acc_rs = {};
    bf16x8 xf[4], kf[4];
#pragma unroll
    for (int sl = 0; sl < 2; ++sl) {
        if (sl == 0) asm volatile("s_waitcnt vmcnt(%0)" ::"n"(6 + YOUNG) : "memory");
        else asm volatile("s_waitcnt vmcnt(%0)" ::"n"(YOUNG) : "memory");
#pragma unroll
        for (int k = 0; k < 4; ++k) {
            xf[k] = lds_b128(xl + sl * 4096 + (((2 * k + h) ^ fj) * 16));
            kf[k] = lds_tr2<128>(kl + (sl * 4 + k) * 512);
        }
        if (isv) {
#pragma unroll
            for (int k = 0; k < 4; ++k) acck = MFMA(xf[k], kf[k], acck);
        } else {
#pragma unroll
            for (int k = 0; k < 4; ++k) acck = MFMA(kf[k], xf[k], acck);
        }
#pragma unroll
        for (int k = 0; k < 4; ++k) {
            accx = MFMA(xf[k], xf[k], accx);
            acc_rs = MFMA16(sel, xf[k], acc_rs);
            acc_ck = MFMA16(sel, kf[k], acc_ck);
        }
        SBAR();
    }
    char* pq = smem + J_W + w * 4096;
    if (isv) {
        if (r < 16) {
#pragma unroll
            for (int q = 0; q < 4; ++q)
                *(f32x4*)(pq + q * 512 + (h * 16 + r) * 16) = (f32x4){acck[4 * q], acck[4 * q + 1], acck[4 * q + 2], acck[4 * q + 3]};
        }
    } else {
        *(f32x4*)(pq + lo16) = (f32x4){acck[0], acck[1], acck[2], acck[3]};
        *(f32x4*)(pq + 1024 + lo16) = (f32x4){acck[4], acck[5], acck[6], acck[7]};
    }
    const float dg = diag32(accx, r);
    if (h == ((r >> 2) & 1)) { ((float*)(pq + 2048))[r] = acc_rs[0]; ((float*)(pq + 2176))[r] = dg; }
    if (lane < 16) ((float*)(pq + 2304))[lane] = acc_ck[0];
    asm volatile("s_waitcnt lgkmcnt(0)\n\ts_barrier" ::: "memory");
    if (w == 0) {
        const char* p0 = smem + J_W;
        float sum = 0.f, sq = 0.f, ck = 0.f;
#pragma unroll
        for (int v = 0; v < 4; ++v) {
            sum += ((const float*)(p0 + v * 4096 + 2048))[r];
            sq += ((const float*)(p0 + v * 4096 + 2176))[r];
            ck += ((const float*)(p0 + v * 4096 + 2304))[lane & 15];
        }
        float mu, rs;
        ln_stat(sum, sq, mu, rs);
        const bf16x8 ex = xext(mu, h), ek = wext(ck, h);
        const __amdgpu_buffer_rsrc_t rkv_st = __builtin_amdgcn_make_buffer_rsrc(ws, 0, 131072 + 16384, 0x00020000);
        if (isv) {
            float* srs = (float*)(smem + J_W + 2432);
            if (h == 0) srs[r] = rs;
#pragma unroll
            for (int v = 1; v < 4; ++v)
#pragma unroll
                for (int q = 0; q < 4; ++q) {
                    const f32x4 o = *(const f32x4*)(p0 + v * 4096 + q * 512 + (h * 16 + (r & 15)) * 16);
                    acck[4 * q] += o[0]; acck[4 * q + 1] += o[1]; acck[4 * q + 2] += o[2]; acck[4 * q + 3] += o[3];
                }
            acck = MFMA(ex, ek, acck);
            f32x16 v;
#pragma unroll
            for (int q = 0; q < 4; ++q) {
                const f32x4 rr = *(const f32x4*)(srs + 8 * q + 4 * h);
#pragma unroll
                for (int i = 0; i < 4; ++i) v[4 * q + i] = acck[4 * q + i] * rr[i];
            }
            const unsigned vo = r < 16 ? (unsigned)WS_VF + h * 256 + r * 16 : 0x40000000u;
            __builtin_amdgcn_raw_buffer_store_b128(pack8(v, 0, 1.0f), rkv_st, vo + rg * 1024, 0, SC1);
            __builtin_amdgcn_raw_buffer_store_b128(pack8(v, 8, 1.0f), rkv_st, vo + rg * 1024 + 512, 0, SC1);
        } else {
#pragma unroll
            for (int v = 1; v < 4; ++v)
#pragma unroll
                for (int q = 0; q < 2; ++q) {
                    const f32x4 o = *(const f32x4*)(p0 + v * 4096 + q * 1024 + lo16);
                    acck[4 * q] += o[0]; acck[4 * q + 1] += o[1]; acck[4 * q + 2] += o[2]; acck[4 * q + 3] += o[3];
                }
            acck = MFMA(ek, ex, acck);
            u32x2 ms = {__float_as_uint(mu), __float_as_uint(rs)};
            __builtin_amdgcn_raw_buffer_store_b64(ms, rkv_st, h == 0 ? (unsigned)WS_STAT + rg * 256 + r * 8 : 0x40000000u, 0, SC1);
            __builtin_amdgcn_raw_buffer_store_b128(pack8(acck, 0, rs), rkv_st, lo16 + rg * 1024, 0, SC1);
        }
    }
}

constexpr int A_STAGE = 16384;
constexpr int VT = 1152, VH = 288;
constexpr int A_SLOT = 8 * 1024 + 8 * VT;
DI void role_a(const u16* __restrict__ x, const u16* __restrict__ wq, char* __restrict__ ws, char* smem, const int bid, const int w, const int lane) {
    const int xcd = bid & 7, idx = bid >> 3;
    const int it = xcd * 4 + (idx & 3), g = idx >> 2;
    const int p = w >> 1, rgl = w & 1;
    const int r = lane & 31, h = lane >> 5;
    const unsigned lds0 = (unsigned)(uintptr_t)smem;
    const unsigned lo16 = lane * 16;
    LAS const char* L = (LAS const char*)smem;
    __builtin_amdgcn_s_setprio(2);

    const int prow = lane >> 3, pslot = lane & 7;
    const unsigned xo0 = (unsigned)((16 * w + prow) * DM * 2 + ((pslot ^ (((16 * w + prow) >> 1) & 7)) * 16));
    const unsigned xo1 = (unsigned)((16 * w + 8 + prow) * DM * 2 + ((pslot ^ (((16 * w + 8 + prow) >> 1) & 7)) * 16));
    const unsigned wo0 = (unsigned)((16 * w + prow) * DM * 2 + ((pslot ^ (((prow >> 1) & 1) << 2)) * 16));
    const unsigned wo1 = wo0 + 8 * DM * 2;
    const char* xb = (const char*)(x + (size_t)it * 64 * DM);
    const char* wb = (const char*)(wq + g * 64);
    const int blk = (lane >> 4) & 1, q4 = (lane & 15) >> 2, p4 = lane & 3;
    const bf16x8 sel = make_sel(lane);
#define A_DMA(s, i)                                                                                       \
    do {                                                                                                  \
        if ((s) < 8) {                                                                                    \
            const unsigned sb_ = lds0 + ((s) & 3) * A_STAGE + w * 2048;                                   \
            if ((i) == 0) glds16s(xb + (s) * 128, xo0, sb_);                                              \
            if ((i) == 1) glds16s(xb + (s) * 128, xo1, sb_ + 1024);                                       \
            if ((i) == 2) glds16s(wb + (s) * (64 * DM * 2), wo0, sb_ + 8192);                             \
            if ((i) == 3) glds16s(wb + (s) * (64 * DM * 2), wo1, sb_ + 8192 + 1024);                      \
        }                                                                                                 \
    } while (0)
#define A_DMA4(s) do { A_DMA(s, 0); A_DMA(s, 1); A_DMA(s, 2); A_DMA(s, 3); } while (0)
    A_DMA4(0); A_DMA4(1); A_DMA4(2); A_DMA4(3);
    const u64 tag = load_epoch(ws) + 1;

    const int fx = (r >> 1) & 7;
    const int wlane = 8192 + h * 1024 + q4 * 128 + (((p * 4 + blk * 2 + (p4 >> 1)) ^ (((q4 >> 1) & 1) << 2)) * 16) + (p4 & 1) * 8;
    f32x16 accq = {};
    f32x4 acc_cs = {};
    bf16x8 xa[2], wc[2];
#define A_READ(set, s, j)                                                             \
    do {                                                                              \
        LAS const char* sb_ = L + ((s) & 3) * A_STAGE;                                \
        wc[set] = lds_tr2<512>(sb_ + wlane + (j) * 2048);                             \
        xa[set] = lds_b128(sb_ + (rgl * 32 + r) * 128 + (((2 * (j) + h) ^ fx) * 16)); \
        SBAR();                                                                       \
    } while (0)
#define A_MMA(set, j, ds, d0)                                                         \
    do {                                                                              \
        accq = MFMA(wc[set], xa[set], accq);                                          \
        acc_cs = MFMA16(sel, wc[set], acc_cs);                                        \
        SBAR();                                                                       \
        A_DMA(ds, d0);                                                                \
    } while (0)
    wait_bar<12>();
    if (lane == 0 && w == 1) set_flag(ws, SY_SEEN + (size_t)bid * 8, tag);
    A_READ(0, 0, 0);
#pragma unroll
    for (int s = 0; s < 8; ++s) {
        A_READ(1, s, 1); A_MMA(0, 0, (s >= 1 ? s + 3 : 8), 1);
        A_READ(0, s, 2); A_MMA(1, 1, (s >= 1 ? s + 3 : 8), 2);
        A_READ(1, s, 3); A_MMA(0, 2, (s >= 1 ? s + 3 : 8), 3);
        if (s < 7) {
            if (s <= 4) wait_bar<8>(); else if (s == 5) wait_bar<4>(); else wait_bar<0>();
            A_READ(0, s + 1, 0);
        }
        A_MMA(1, 3, s + 4, 0);
    }
#undef A_READ
#undef A_MMA
#undef A_DMA
#undef A_DMA4

    const __amdgpu_buffer_rsrc_t rkv = __builtin_amdgcn_make_buffer_rsrc(ws, 0, 131072, 0x00020000);
    gu64* kvf = (gu64*)(ws + WS_SYNC + SY_KV +
                        (size_t)(lane < 32 ? ((lane >> 2) * 8 + 2 * w) * 2 + (lane & 3) : (2 * it + rgl) * 2) * FL);
    unsigned long long rdy = 0;
    for (unsigned spins = 0; spins < SPIN_CAP; ++spins) {
        rdy = __ballot(__hip_atomic_load(kvf, __ATOMIC_RELAXED, __HIP_MEMORY_SCOPE_AGENT) == tag);
        if ((rdy & 0x100000FFFull) == 0x100000FFFull) break;
        __builtin_amdgcn_s_sleep(1);
    }
    __builtin_amdgcn_fence(__ATOMIC_ACQUIRE, "wavefront");
    asm volatile("" ::: "memory");
    bf16x8 qa, qb;
    const __amdgpu_buffer_rsrc_t rst = __builtin_amdgcn_make_buffer_rsrc(ws + WS_STAT + (2 * it + rgl) * 256, 0, 256, 0x00020000);
    const u32x2 stq = __builtin_amdgcn_raw_buffer_load_b64(rst, r * 8, 0, SC1);
    u32x4 sk0, sk1, sv0, sv1;
#define KV_LOAD(c)                                                                                     \
    do {                                                                                               \
        const int so_ = ((c) * 8 + 2 * w) * 1024;                                                      \
        sk0 = __builtin_amdgcn_raw_buffer_load_b128(rkv, lo16, so_, SC1);                              \
        sk1 = __builtin_amdgcn_raw_buffer_load_b128(rkv, lo16 + 1024, so_, SC1);                       \
        sv0 = __builtin_amdgcn_raw_buffer_load_b128(rkv, lo16 + (unsigned)WS_VF, so_, SC1);            \
        sv1 = __builtin_amdgcn_raw_buffer_load_b128(rkv, lo16 + (unsigned)WS_VF + 1024, so_, SC1);     \
    } while (0)
    const int kwr = w * 2048 + lo16, vwr = 8192 + w * 2 * VT + (lane >> 4) * VH + (lane & 15) * 16;
#define KV_WRITE(slotbase)                                                                             \
    do {                                                                                               \
        *(LAS u32x4*)(slotbase + kwr) = sk0; *(LAS u32x4*)(slotbase + kwr + 1024) = sk1;               \
        *(LAS u32x4*)(slotbase + vwr) = sv0; *(LAS u32x4*)(slotbase + vwr + VT) = sv1;                 \
    } while (0)
    LAS char* LW = (LAS char*)smem;
#define KV_CONST(s3)                                                                                   \
    do {                                                                                               \
        const unsigned c_ = (lane & 32) ? 0u : 0x3F803F80u;                                            \
        *(LAS u32x4*)(LW + (s3) * A_SLOT + 8192 + (lane & 31) * VH + 256 + ((lane & 32) >> 1)) = (u32x4){c_, c_, c_, c_}; \
    } while (0)
    {
        u32x4 tk0, tk1, tv0, tv1, uk0, uk1, uv0, uv1;
        KV_LOAD(0);
        tk0 = sk0; tk1 = sk1; tv0 = sv0; tv1 = sv1;
        KV_LOAD(1);
        uk0 = sk0; uk1 = sk1; uv0 = sv0; uv1 = sv1;
        KV_LOAD(2);
        SBAR();
        if (threadIdx.x < 128) KV_CONST(threadIdx.x >> 6);
        accq = MFMA(wext(acc_cs[0], h), xext(__uint_as_float(stq[0]), h), accq);
        const float qs = __uint_as_float(stq[1]) * (0.25f * 1.4426950408889634f);
        qa = BC8(pack8(accq, 0, qs));
        qb = BC8(pack8(accq, 8, qs));
        SBAR();
        *(LAS u32x4*)(LW + kwr) = tk0; *(LAS u32x4*)(LW + kwr + 1024) = tk1;
        *(LAS u32x4*)(LW + vwr) = tv0; *(LAS u32x4*)(LW + vwr + VT) = tv1;
        *(LAS u32x4*)(LW + A_SLOT + kwr) = uk0; *(LAS u32x4*)(LW + A_SLOT + kwr + 1024) = uk1;
        *(LAS u32x4*)(LW + A_SLOT + vwr) = uv0; *(LAS u32x4*)(LW + A_SLOT + vwr + VT) = uv1;
    }
    asm volatile("s_waitcnt lgkmcnt(0)\n\ts_barrier" ::: "memory");
    if (w == 2) KV_CONST(2);
    const int kln = lo16, vln = 8192 + h * VH + (r < 17 ? r : 17) * 16;
    f32x16 nma, nmb;
    bf16x8 kf = lds_b128(L + kln);
    bf16x8 v0 = lds_b128(L + vln), v1 = lds_b128(L + vln + 2 * VH);
    {
        f32x16 z = {};
        f32x16 s0 = MFMA(kf, qa, z), s1 = MFMA(kf, qb, z);
        const float ma = swap_max(max16(s0)), mb = swap_max(max16(s1));
#pragma unroll
        for (int i = 0; i < 16; ++i) { nma[i] = -ma; nmb[i] = -mb; }
    }
    f32x16 oa = {}, ob = {};
    f32x16 sa = MFMA(kf, qa, nma), sb;
    float e0, e1;
    u32x4 pp = {0u, 0u, 0u, 0u};
    bf16x8 vq = v1;
    e0 = EXP2(sa[0]); e1 = EXP2(sa[1]);
#define ATT_HALF(SX, OX, OP, SY, NEXT, V0, V1, LOADS)                                                   \
    do {                                                                                                \
        OP = MFMA(vq, BC8(pp), OP); SBAR();                                                             \
        float e2 = EXP2(SX[2]), e3 = EXP2(SX[3]); LOADS; float e4 = EXP2(SX[4]), e5 = EXP2(SX[5]); SBAR(); \
        SY = NEXT; SBAR();                                                                              \
        float e6 = EXP2(SX[6]), e7 = EXP2(SX[7]);                                                       \
        u32x4 p0; p0[0] = cvtpk(e0, e1); p0[1] = cvtpk(e2, e3); p0[2] = cvtpk(e4, e5); p0[3] = cvtpk(e6, e7); SBAR(); \
        e0 = EXP2(SX[8]); e1 = EXP2(SX[9]); SBAR();                                                     \
        OX = MFMA(V0, BC8(p0), OX); SBAR();                                                             \
        e2 = EXP2(SX[10]); e3 = EXP2(SX[11]); e4 = EXP2(SX[12]); e5 = EXP2(SX[13]); e6 = EXP2(SX[14]); e7 = EXP2(SX[15]); \
        pp[0] = cvtpk(e0, e1); pp[1] = cvtpk(e2, e3); pp[2] = cvtpk(e4, e5); pp[3] = cvtpk(e6, e7); vq = V1; SBAR(); \
        e0 = EXP2(SY[0]); e1 = EXP2(SY[1]); SBAR();                                                     \
    } while (0)
#define ATT_TILE(KN, VN, EXTRA)                                                                         \
    do {                                                                                                \
        bf16x8 kn, n0, n1;                                                                              \
        ATT_HALF(sa, oa, ob, sb, MFMA(kf, qb, nmb), v0, v1, kn = lds_b128(KN); EXTRA);                  \
        ATT_HALF(sb, ob, oa, sa, MFMA(kn, qa, nma), v0, v1, (n0 = lds_b128(VN), n1 = lds_b128((VN) + 2 * VH))); \
        kf = kn; v0 = n0; v1 = n1;                                                                      \
    } while (0)
    __builtin_amdgcn_s_setprio(1);
    int cur = 0, nxt = A_SLOT, fre = 2 * A_SLOT;
#pragma unroll 1
    for (int c = 0; c < 8; ++c) {
        LAS const char* kc = L + cur + kln;
        LAS const char* vc = L + cur + vln;
        u64 fv = tag;
        if (rdy != ~0ull) fv = __hip_atomic_load(kvf, __ATOMIC_RELAXED, __HIP_MEMORY_SCOPE_AGENT);
        SBAR();
        ATT_TILE(kc + 1 * 1024, vc + 1 * VT, (void)0);
        ATT_TILE(kc + 2 * 1024, vc + 2 * VT, KV_WRITE((LW + fre)));
        if (rdy != ~0ull) {
            rdy = __ballot(fv == tag);
            const unsigned long long need = c < 5 ? 0xFull << (4 * (c + 3)) : 0ull;
            for (unsigned spins = 0; (rdy & need) != need && spins < SPIN_CAP; ++spins)
                rdy = __ballot(__hip_atomic_load(kvf, __ATOMIC_RELAXED, __HIP_MEMORY_SCOPE_AGENT) == tag);
            __builtin_amdgcn_fence(__ATOMIC_ACQUIRE, "wavefront");
            asm volatile("" ::: "memory");
        }
        SBAR();
        ATT_TILE(kc + 3 * 1024, vc + 3 * VT, KV_LOAD(c + 3));
        ATT_TILE(kc + 4 * 1024, vc + 4 * VT, (void)0);
        ATT_TILE(kc + 5 * 1024, vc + 5 * VT, (void)0);
        ATT_TILE(kc + 6 * 1024, vc + 6 * VT, (void)0);
        ATT_TILE(kc + 7 * 1024, vc + 7 * VT, (void)0);
        ATT_TILE(L + nxt + kln, L + nxt + vln, (void)0);
        asm volatile("s_waitcnt lgkmcnt(0)\n\ts_barrier" ::: "memory");
        const int t_ = cur; cur = nxt; nxt = fre; fre = t_;
    }
    ob = MFMA(vq, BC8(pp), ob);
#undef ATT_TILE
#undef ATT_HALF
#undef KV_LOAD
#undef KV_WRITE
#undef KV_CONST
    float la, lb;
    {
        la = swap_add(oa[8]); lb = swap_add(ob[8]);
        const bool bad = !(la > 9.094947e-13f && la < 1.0995116e12f && lb > 9.094947e-13f && lb < 1.0995116e12f);
        if (__builtin_expect(__any(bad), 0)) {
            oa = attn_slow(rkv, qa, lane); ob = attn_slow(rkv, qb, lane);
            la = swap_add(oa[8]); lb = swap_add(ob[8]);
        }
    }
    const __amdgpu_buffer_rsrc_t ro = __builtin_amdgcn_make_buffer_rsrc(
        ws + WS_ACT + ((size_t)(2 * it + rgl) * NKS + 4 * g + 2 * p) * 1024, 0, 2048, 0x00020000);
    __builtin_amdgcn_raw_buffer_store_b128(pack8(oa, 0, __builtin_amdgcn_rcpf(la)), ro, lo16, 0, SC1);
    __builtin_amdgcn_raw_buffer_store_b128(pack8(ob, 0, __builtin_amdgcn_rcpf(lb)), ro, lo16 + 1024, 0, SC1);
    asm volatile("s_waitcnt vmcnt(0)\n\ts_barrier" ::: "memory");
    if (threadIdx.x == 0) set_flag(ws, SY_O + (size_t)(it * 8 + g) * FL, tag);
}

constexpr int G_W = 32768;
DI void g_gateup(const u16* __restrict__ x, const u16* __restrict__ wg, const u16* __restrict__ wu, char* __restrict__ ws,
                 char* smem, const int bid, const int rt, const int cg, const int job, const int w, const int lane, u64& tag) {
    const int wr = w & 1, wn = w >> 1;
    const int r = lane & 31, h = lane >> 5;
    const unsigned lds0 = (unsigned)(uintptr_t)smem;
    const unsigned lo16 = lane * 16;
    LAS const char* L = (LAS const char*)smem;
    const int prow = lane >> 3, pslot = lane & 7;
    const unsigned xoA = (unsigned)((32 * w + prow) * DM * 2 + ((pslot ^ (prow >> 1)) * 16));
    const unsigned xoB = (unsigned)((32 * w + 8 + prow) * DM * 2 + ((pslot ^ (4 + (prow >> 1))) * 16));
    const unsigned wo0 = (unsigned)((8 * w + prow) * DF * 2 + ((pslot ^ (((prow >> 1) & 1) << 2)) * 16));
    const unsigned wo1 = wo0 + 128;
    const char* xb = (const char*)(x + (size_t)rt * 128 * DM);
    const char* wA = (const char*)(wg + cg * 128);
    const char* wB = (const char*)(wu + cg * 128);
#define G_DMAX(j, i)                                                                                      \
    do {                                                                                                  \
        if ((j) < 8) glds16s(xb + (j) * 128 + ((i) >> 1) * (16 * DM * 2), ((i) & 1) ? xoB : xoA,          \
                             lds0 + ((j) & 1) * 16384 + w * 4096 + (i) * 1024);                           \
    } while (0)
#define G_DMAW(u, i)                                                                                      \
    do {                                                                                                  \
        if ((u) < 16) glds16s((((i) >> 1) ? wB : wA) + (size_t)(u) * (32 * DF * 2), ((i) & 1) ? wo1 : wo0, \
                              lds0 + G_W + ((u) % 3) * 16384 + w * 4096 + (i) * 1024);                    \
    } while (0)
#pragma unroll
    for (int i = 0; i < 4; ++i) G_DMAX(0, i);
#pragma unroll
    for (int i = 0; i < 4; ++i) G_DMAW(0, i);
    tag = load_epoch(ws) + 1;
    if (job >= 0) kv_job<8>(ws, smem, job, w, lane);
#pragma unroll
    for (int i = 0; i < 4; ++i) G_DMAW(1, i);

    const int ga = 2 * wr + wn, gb = 2 * wr + 1 - wn;
    const int fx = (r >> 1) & 7;
    const int blk = (lane >> 4) & 1, q4 = (lane & 15) >> 2, p4 = lane & 3;
    const int wl0 = G_W + h * 4096 + wn * 1024 + q4 * 128 + (((blk * 2 + (p4 >> 1)) ^ (((q4 >> 1) & 1) << 2)) * 16) + (p4 & 1) * 8;
    const int wl1 = wl0 ^ 64;
    const int c0 = wl0 + wr * 2048, c1 = wl1 + wr * 2048, d0 = wl0 + (1 - wr) * 2048, d1 = wl1 + (1 - wr) * 2048;
    f32x16 ac0a = {}, ac0b = {}, ac1a = {}, ac1b = {}, ad0a = {}, ad0b = {}, ad1a = {}, ad1b = {};
    bf16x8 xa[2], xbf[2], fc0[2], fc1[2], fd0[2], fd1[2];
#define G_XS(u, j) (L + (((u) >> 1) & 1) * 16384 + r * 128 + (((2 * (((u) & 1) * 2 + (j)) + h) ^ fx) * 16))
#define G_WO(u, j) (((u) % 3) * 16384 + (j) * 8192)
#define G_READ(set, u, j)                                                             \
    do {                                                                              \
        fc0[set] = lds_tr2<512>(L + c0 + G_WO(u, j));                                 \
        xa[set] = lds_b128(G_XS(u, j) + ga * 4096);                                   \
        xbf[set] = lds_b128(G_XS(u, j) + gb * 4096);                                  \
        fc1[set] = lds_tr2<512>(L + c1 + G_WO(u, j));                                 \
        fd0[set] = lds_tr2<512>(L + d0 + G_WO(u, j));                                 \
        fd1[set] = lds_tr2<512>(L + d1 + G_WO(u, j));                                 \
        SBAR();                                                                       \
    } while (0)
#define G_STEP(set, RD, nu, nj, D0, D1, D2, D3)                                       \
    do {                                                                              \
        ac0a = MFMA(fc0[set], xa[set], ac0a); SBAR();                                 \
        if (RD) { fc0[1 - set] = lds_tr2<512>(L + c0 + G_WO(nu, nj)); SBAR(); }       \
        ac0b = MFMA(fc0[set], xbf[set], ac0b); SBAR();                                \
        if (RD) { xa[1 - set] = lds_b128(G_XS(nu, nj) + ga * 4096); xbf[1 - set] = lds_b128(G_XS(nu, nj) + gb * 4096); SBAR(); } \
        ac1a = MFMA(fc1[set], xa[set], ac1a); SBAR();                                 \
        if (RD) { fc1[1 - set] = lds_tr2<512>(L + c1 + G_WO(nu, nj)); SBAR(); }       \
        ac1b = MFMA(fc1[set], xbf[set], ac1b); SBAR();                                \
        if (RD) { fd0[1 - set] = lds_tr2<512>(L + d0 + G_WO(nu, nj)); SBAR(); }       \
        ad0a = MFMA(fd0[set], xa[set], ad0a); SBAR();                                 \
        if (RD) { fd1[1 - set] = lds_tr2<512>(L + d1 + G_WO(nu, nj)); SBAR(); }       \
        D2;                                                                           \
        ad0b = MFMA(fd0[set], xbf[set], ad0b); SBAR();                                \
        D0;                                                                           \
        ad1a = MFMA(fd1[set], xa[set], ad1a); SBAR();                                 \
        D1;                                                                           \
        ad1b = MFMA(fd1[set], xbf[set], ad1b); SBAR();                                \
        D3;                                                                           \
    } while (0)
    wait_bar<4>();
    if (job >= 0 && threadIdx.x == 0) set_flag(ws, SY_KV + (size_t)job * FL, tag);
    __builtin_amdgcn_s_setprio(0);
    if (lane == 0 && w == 1) set_flag(ws, SY_SEEN + (size_t)(256 + bid) * 8, tag);
    G_READ(0, 0, 0);
    G_DMAW(2, 0); G_DMAW(2, 1); G_DMAX(1, 0); G_DMAX(1, 1);
#pragma unroll
    for (int u = 0; u < 16; ++u) {
        if ((u & 1) == 0) G_STEP(0, true, u, 1, G_DMAW(u + 2, 2), G_DMAW(u + 2, 3), G_DMAX(u / 2 + 1, 2), G_DMAX(u / 2 + 1, 3));
        else              G_STEP(0, true, u, 1, G_DMAW(u + 2, 2), G_DMAW(u + 2, 3), (void)0, (void)0);
        if (u < 15) {
            const int nfly = (u + 2 < 16 ? 4 : 0) + ((((u & 1) == 0) && (u / 2 + 1 < 8)) ? 4 : 0);
            if (nfly == 8) wait_bar<8>(); else if (nfly == 4) wait_bar<4>(); else wait_bar<0>();
        }
        if (u & 1) G_STEP(1, u < 15, u + 1, 0, G_DMAW(u + 3, 0), G_DMAW(u + 3, 1), G_DMAX((u + 1) / 2 + 1, 0), G_DMAX((u + 1) / 2 + 1, 1));
        else       G_STEP(1, true, u + 1, 0, G_DMAW(u + 3, 0), G_DMAW(u + 3, 1), (void)0, (void)0);
    }
#undef G_STEP
#undef G_XS
#undef G_WO
#undef G_READ
#undef G_DMAX
#undef G_DMAW
    wait_flags(ws, (lane & 2) ? (unsigned)(SY_C + (cg + 14 * (lane & 1)) * FL) : (unsigned)(SY_KV + (rt * 4 + 2 * wr + (lane & 1)) * 2 * FL), tag);
    const __amdgpu_buffer_rsrc_t rst = __builtin_amdgcn_make_buffer_rsrc(ws + WS_STAT, 0, 32768, 0x00020000);
    const u32x2 sta = __builtin_amdgcn_raw_buffer_load_b64(rst, rt * 1024 + ga * 256 + r * 8, 0, SC1);
    const u32x2 stb = __builtin_amdgcn_raw_buffer_load_b64(rst, rt * 1024 + gb * 256 + r * 8, 0, SC1);
    const unsigned co_c = 16384 + ((wr * 14 + cg) * 128 + wn * 64 + r) * 4, co_d = 16384 + (((1 - wr) * 14 + cg) * 128 + wn * 64 + r) * 4;
    const float csc0 = __uint_as_float(__builtin_amdgcn_raw_buffer_load_b32(rst, co_c, 0, SC1));
    const float csc1 = __uint_as_float(__builtin_amdgcn_raw_buffer_load_b32(rst, co_c + 128, 0, SC1));
    const float csd0 = __uint_as_float(__builtin_amdgcn_raw_buffer_load_b32(rst, co_d, 0, SC1));
    const float csd1 = __uint_as_float(__builtin_amdgcn_raw_buffer_load_b32(rst, co_d + 128, 0, SC1));
    const float mu_a = __uint_as_float(sta[0]), rs_a = __uint_as_float(sta[1]), mu_b = __uint_as_float(stb[0]), rs_b = __uint_as_float(stb[1]);
    {
        const bf16x8 ea = xext(mu_a, h), eb = xext(mu_b, h);
        const bf16x8 ec0 = wext(csc0, h), ec1 = wext(csc1, h), ed0 = wext(csd0, h), ed1 = wext(csd1, h);
        ac0a = MFMA(ec0, ea, ac0a); ad0a = MFMA(ed0, ea, ad0a);
        ac0b = MFMA(ec0, eb, ac0b); ad0b = MFMA(ed0, eb, ad0b);
        ac1a = MFMA(ec1, ea, ac1a); ad1a = MFMA(ed1, ea, ad1a);
        ac1b = MFMA(ec1, eb, ac1b); ad1b = MFMA(ed1, eb, ad1b);
    }
    const __amdgpu_buffer_rsrc_t rh = __builtin_amdgcn_make_buffer_rsrc(
        ws + WS_ACT + ((size_t)rt * 4 * NKS + 32 + 8 * cg + 4 * wn) * 1024, 0, 4 * NKS * 1024, 0x00020000);
    const float K1c = 2.0f * 0.7978845608028654f * 1.4426950408889634f, K3c = K1c * 0.044715f;
    auto geglu = [&](const f32x16& G, const f32x16& U, float rs, int so) {
        typedef float f32x2 __attribute__((ext_vector_type(2)));
        const float c1_ = -K1c * rs, c3_ = -K3c * rs * rs * rs, ri2 = __builtin_amdgcn_rcpf(rs * rs);
        const f32x2 c1v = {c1_, c1_}, c3v = {c3_, c3_}, riv = {ri2, ri2};
        u32x4 o0, o1;
#pragma unroll
        for (int i = 0; i < 8; ++i) {
            const f32x2 gg = {G[2 * i], G[2 * i + 1]}, uu = {U[2 * i], U[2 * i + 1]};
            const f32x2 t = gg * __builtin_elementwise_fma(gg * gg, c3v, c1v);
            const f32x2 e = {EXP2(t[0]), EXP2(t[1])};
            const f32x2 d = __builtin_elementwise_fma(e, riv, riv);
            const f32x2 rr = {__builtin_amdgcn_rcpf(d[0]), __builtin_amdgcn_rcpf(d[1])};
            const f32x2 hv = (gg * uu) * rr;
            const unsigned pk = cvtpk(hv[0], hv[1]);
            if (i < 4) o0[i] = pk; else o1[i - 4] = pk;
        }
        __builtin_amdgcn_raw_buffer_store_b128(o0, rh, lo16 + so, 0, SC1);
        __builtin_amdgcn_raw_buffer_store_b128(o1, rh, lo16 + so + 1024, 0, SC1);
    };
    const int soa = ga * NKS * 1024, sob = gb * NKS * 1024;
    const int nlow = job >= 0 ? 2 : 3;
#define GE_PRIO(i) do { if (nlow == (i)) __builtin_amdgcn_s_setprio(2); } while (0)
#define GE_HALF()                                                                                        \
    do {                                                                                                 \
        asm volatile("s_waitcnt vmcnt(2)\n\ts_barrier" ::: "memory");     \
        if (threadIdx.x == 0) set_flag(ws, SY_H + (size_t)(rt * 16 + cg) * FL, tag);                     \
    } while (0)
    if (wr == 0) {
        GE_PRIO(0); geglu(ac0a, ad0a, rs_a, soa);        GE_PRIO(1); geglu(ac0b, ad0b, rs_b, sob);
        GE_PRIO(2); geglu(ac1a, ad1a, rs_a, soa + 2048); GE_HALF(); GE_PRIO(3); geglu(ac1b, ad1b, rs_b, sob + 2048);
    } else {
        GE_PRIO(0); geglu(ad0a, ac0a, rs_a, soa);        GE_PRIO(1); geglu(ad0b, ac0b, rs_b, sob);
        GE_PRIO(2); geglu(ad1a, ac1a, rs_a, soa + 2048); GE_HALF(); GE_PRIO(3); geglu(ad1b, ac1b, rs_b, sob + 2048);
    }
#undef GE_HALF
#undef GE_PRIO
    asm volatile("s_waitcnt vmcnt(0) lgkmcnt(0)\n\ts_barrier" ::: "memory");
    if (threadIdx.x == 0) set_flag(ws, SY_H + (size_t)(rt * 16 + cg) * FL + 64, tag);
}

DI void g_retile(const u16* __restrict__ wo, const u16* __restrict__ wd, const u16* __restrict__ wg, const u16* __restrict__ wu,
                 char* __restrict__ ws, char* smem, const int bid,
                 const int j, const int job, const int w, const int lane, u64& tag) {
    const int r = lane & 31, h = lane >> 5;
    const int ng = j >> 1, ks0 = 72 * (j & 1) + 18 * w;
    const __amdgpu_buffer_rsrc_t rw =
        __builtin_amdgcn_make_buffer_rsrc(ws + WS_W2F + ((size_t)ng * NKS + ks0) * 1024, 0, 18 * 1024, 0x00020000);
    tag = load_epoch(ws) + 1;
    if (job >= 0) kv_job<0>(ws, smem, job, w, lane);
    if (job >= 0 && w == 0) {
        asm volatile("s_waitcnt vmcnt(0)" ::: "memory");
        if (lane == 0) set_flag(ws, SY_KV + (size_t)job * FL, tag);
    }
    if (lane == 0 && w == 1) set_flag(ws, SY_SEEN + (size_t)(256 + bid) * 8, tag);
    if (j < 28) {
        const int t = threadIdx.x;
        const char* src = (const char*)((j < 14 ? wg : wu) + (j < 14 ? j : j - 14) * 128) + (size_t)(t >> 4) * 32 * (DF * 2) + (t & 15) * 16;
        float cs[8] = {0.f, 0.f, 0.f, 0.f, 0.f, 0.f, 0.f, 0.f};
#pragma unroll 1
        for (int b = 0; b < 2; ++b) {
            u32x4 v[16];
#pragma unroll
            for (int i = 0; i < 16; ++i) v[i] = *(const u32x4*)(src + (size_t)(b * 16 + i) * (DF * 2));
#pragma unroll
            for (int i = 0; i < 16; ++i)
#pragma unroll
                for (int d = 0; d < 4; ++d) {
                    const unsigned u = v[i][d];
                    cs[2 * d] += __uint_as_float(u << 16);
                    cs[2 * d + 1] += __uint_as_float(u & 0xffff0000u);
                }
        }
        *(f32x4*)(smem + t * 32) = (f32x4){cs[0], cs[1], cs[2], cs[3]};
        *(f32x4*)(smem + t * 32 + 16) = (f32x4){cs[4], cs[5], cs[6], cs[7]};
        __syncthreads();
        if (w == 0) {
            float s0 = 0.f, s1 = 0.f;
#pragma unroll
            for (int k = 0; k < 16; ++k) { s0 += ((const float*)smem)[k * 128 + 2 * lane]; s1 += ((const float*)smem)[k * 128 + 2 * lane + 1]; }
            const __amdgpu_buffer_rsrc_t rc = __builtin_amdgcn_make_buffer_rsrc(ws + WS_CS + j * 512, 0, 512, 0x00020000);
            __builtin_amdgcn_raw_buffer_store_b64((u32x2){__float_as_uint(s0), __float_as_uint(s1)}, rc, lane * 8, 0, SC1);
            asm volatile("s_waitcnt vmcnt(0)" ::: "memory");
            if (lane == 0) set_flag(ws, SY_C + (size_t)j * FL, tag);
        }
    }
#pragma unroll 1
    for (int b = 0; b < 3; ++b) {
        u16 v[6][8];
#pragma unroll
        for (int f = 0; f < 6; ++f) {
            const int ks = ks0 + b * 6 + f;
            const u16* src = (ks < 32 ? wo + (size_t)ks * 16 * DM : wd + (size_t)(ks - 32) * 16 * DM) + ng * 32 + r + h * 4 * DM;
#pragma unroll
            for (int e = 0; e < 8; ++e) v[f][e] = src[(8 * (e >> 2) + (e & 3)) * DM];
        }
#pragma unroll
        for (int f = 0; f < 6; ++f) {
            u32x4 o;
#pragma unroll
            for (int i = 0; i < 4; ++i) o[i] = (unsigned)v[f][2 * i] | ((unsigned)v[f][2 * i + 1] << 16);
            __builtin_amdgcn_raw_buffer_store_b128(o, rw, lane * 16 + (b * 6 + f) * 1024, 0, SC1);
        }
    }
    asm volatile("s_waitcnt vmcnt(0)\n\ts_barrier" ::: "memory");
    if (threadIdx.x == 0) set_flag(ws, SY_W + (size_t)j * FL, tag);
    if (j == 0 && w == 0) {
#pragma unroll 1
        for (int i = 0; i < 8; ++i) wait_flags(ws, SY_SEEN + i * 512 + lane * 8, tag);
        if (lane == 0) set_flag(ws, SY_EPOCH, tag);
    }
}

DI void role_g(const u16* __restrict__ x, const u16* __restrict__ wk, const u16* __restrict__ wv, const u16* __restrict__ wo, const u16* __restrict__ wg, const u16* __restrict__ wu,
               const u16* __restrict__ wd, char* __restrict__ ws, u16* __restrict__ out, char* smem, const int bid, const int w,
               const int lane) {
    const int xcd = bid & 7, idx = bid >> 3;
    const int q_ = xcd >> 1, l_ = idx >> 3;
    const int rt = (xcd & 1) * 8 + (idx & 7), cg = q_ < 2 ? 4 * q_ + l_ : (l_ < 3 ? 2 + 3 * q_ + l_ : 14), lj = (xcd - 4) * 8 + (idx & 7);
    const int it = xcd * 4 + (idx & 3), g = idx >> 2;
    const unsigned lo16 = lane * 16;
    u64 tag;
    __builtin_amdgcn_s_setprio(3);
    const int job = idx >= 16 ? xcd * 16 + (idx - 16) : -1;
    if (job >= 0) kv_issue(x, (job & 1) ? wv : wk, smem, job, w, lane);
    if (cg < 14) g_gateup(x, wg, wu, ws, smem, bid, rt, cg, job, w, lane, tag);
    else g_retile(wo, wd, wg, wu, ws, smem, bid, lj, job, w, lane, tag);
    __builtin_amdgcn_s_setprio(0);

    const unsigned hfo = (unsigned)(SY_H + ((it >> 1) * 16 + ((7 * w) >> 1) + (lane & 3)) * FL);
    wait_flags(ws, lane < 8 ? hfo : (unsigned)(SY_W + (4 * g + (lane & 3)) * FL), tag);
    const __amdgpu_buffer_rsrc_t ra =
        __builtin_amdgcn_make_buffer_rsrc(ws + WS_ACT + (size_t)(2 * it) * NKS * 1024, 0, 2 * NKS * 1024, 0x00020000);
    const __amdgpu_buffer_rsrc_t rw =
        __builtin_amdgcn_make_buffer_rsrc(ws + WS_W2F + (size_t)(2 * g) * NKS * 1024, 0, 2 * NKS * 1024, 0x00020000);
    const int so = (32 + 28 * w) * 1024;
    constexpr int PD = 6;
    f32x16 acc00 = {}, acc01 = {}, acc10 = {}, acc11 = {};
    u32x4 fw0[8], fw1[8], fa0[8], fa1[8];
#define G_LOAD(i, k)                                                                                   \
    do {                                                                                               \
        fw0[i] = __builtin_amdgcn_raw_buffer_load_b128(rw, lo16, so + (k) * 1024, SC1);                \
        fa0[i] = __builtin_amdgcn_raw_buffer_load_b128(ra, lo16, so + (k) * 1024, SC1);                \
        fa1[i] = __builtin_amdgcn_raw_buffer_load_b128(ra, lo16, so + (NKS + (k)) * 1024, SC1);        \
        fw1[i] = __builtin_amdgcn_raw_buffer_load_b128(rw, lo16, so + (NKS + (k)) * 1024, SC1);        \
        SBAR();                                                                                        \
    } while (0)
#define G_MMA4(i)                                                                                      \
    do {                                                                                               \
        acc00 = MFMA(BC8(fw0[i]), BC8(fa0[i]), acc00); acc01 = MFMA(BC8(fw0[i]), BC8(fa1[i]), acc01);  \
        acc10 = MFMA(BC8(fw1[i]), BC8(fa0[i]), acc10); acc11 = MFMA(BC8(fw1[i]), BC8(fa1[i]), acc11);  \
        SBAR();                                                                                        \
    } while (0)
#define KORD(n) ((n) < 14 ? ((n) >> 1) * 4 + ((n) & 1) : (((n) - 14) >> 1) * 4 + 2 + ((n) & 1))
#pragma unroll
    for (int i = 0; i < PD; ++i) G_LOAD(i, KORD(i));
#pragma unroll
    for (int k = 0; k < 28; ++k) {
        G_MMA4(k % PD);
        if (k + PD == 14) wait_flags(ws, hfo + 64, tag);
        if (k + PD < 28) G_LOAD(k % PD, KORD(k + PD));
    }
#undef KORD
#pragma unroll
    for (int i = 0; i < 8; ++i) {
        fw0[i] = __builtin_amdgcn_raw_buffer_load_b128(rw, lo16, (8 * w + i) * 1024, SC1);
        fw1[i] = __builtin_amdgcn_raw_buffer_load_b128(rw, lo16, (NKS + 8 * w + i) * 1024, SC1);
    }
    u32x4 xv[2];
#pragma unroll
    for (int u = 0; u < 2; ++u) {
        const int t2 = threadIdx.x + 256 * u;
        xv[u] = *(const u32x4*)(x + (size_t)(it * 64 + (t2 >> 3)) * DM + g * 64 + (t2 & 7) * 8);
    }
    wait_flags(ws, (unsigned)(SY_O + (it * 8 + 2 * w + (lane & 1)) * FL), tag);
    __builtin_amdgcn_s_setprio(2);
#pragma unroll
    for (int i = 0; i < 8; ++i) {
        fa0[i] = __builtin_amdgcn_raw_buffer_load_b128(ra, lo16, (8 * w + i) * 1024, SC1);
        fa1[i] = __builtin_amdgcn_raw_buffer_load_b128(ra, lo16, (NKS + 8 * w + i) * 1024, SC1);
    }
    SBAR();
#pragma unroll
    for (int i = 0; i < 8; ++i) G_MMA4(i);
#undef G_LOAD
#undef G_MMA4
    char* dst = smem + w * 16640 + lo16;
#pragma unroll
    for (int q = 0; q < 4; ++q) {
        *(f32x4*)(dst + (0 + q) * 1040) = (f32x4){acc00[4 * q], acc00[4 * q + 1], acc00[4 * q + 2], acc00[4 * q + 3]};
        *(f32x4*)(dst + (4 + q) * 1040) = (f32x4){acc01[4 * q], acc01[4 * q + 1], acc01[4 * q + 2], acc01[4 * q + 3]};
        *(f32x4*)(dst + (8 + q) * 1040) = (f32x4){acc10[4 * q], acc10[4 * q + 1], acc10[4 * q + 2], acc10[4 * q + 3]};
        *(f32x4*)(dst + (12 + q) * 1040) = (f32x4){acc11[4 * q], acc11[4 * q + 1], acc11[4 * q + 2], acc11[4 * q + 3]};
    }
    const __amdgpu_buffer_rsrc_t rout = __builtin_amdgcn_make_buffer_rsrc(out + (size_t)it * 64 * DM + g * 64, 0, 63 * DM * 2 + 128, 0x00020000);
    __syncthreads();
#pragma unroll
    for (int u = 0; u < 2; ++u) {
        const int t2 = threadIdx.x + 256 * u, row = t2 >> 3, c8 = t2 & 7;
        const int a = c8 >> 2, q = c8 & 3, b = row >> 5, rr = row & 31;
        const char* s0 = smem + ((a * 2 + b) * 4 + q) * 1040 + rr * 16;
        f32x4 lo = {__uint_as_float(xv[u][0] << 16), __uint_as_float(xv[u][0] & 0xffff0000u), __uint_as_float(xv[u][1] << 16), __uint_as_float(xv[u][1] & 0xffff0000u)};
        f32x4 hi = {__uint_as_float(xv[u][2] << 16), __uint_as_float(xv[u][2] & 0xffff0000u), __uint_as_float(xv[u][3] << 16), __uint_as_float(xv[u][3] & 0xffff0000u)};
#pragma unroll
        for (int gw = 0; gw < 4; ++gw) {
            lo += *(const f32x4*)(s0 + gw * 16640);
            hi += *(const f32x4*)(s0 + gw * 16640 + 512);
        }
        u32x4 o = {cvtpk(lo[0], lo[1]), cvtpk(lo[2], lo[3]), cvtpk(hi[0], hi[1]), cvtpk(hi[2], hi[3])};
        __builtin_amdgcn_raw_buffer_store_b128(o, rout, (row * DM + c8 * 8) * 2, 0, SC1);
    }
}

__global__ __launch_bounds__(256, 2) void fused_block(const u16* __restrict__ x, const u16* __restrict__ wq,
                                                      const u16* __restrict__ wk, const u16* __restrict__ wv,
                                                      const u16* __restrict__ wo, const u16* __restrict__ wg,
                                                      const u16* __restrict__ wu, const u16* __restrict__ wd,
                                                      char* __restrict__ ws, u16* __restrict__ out) {
    __shared__ __attribute__((aligned(16))) char smem[81920];
    const int lane = threadIdx.x & 63;
    const int w = __builtin_amdgcn_readfirstlane(threadIdx.x >> 6);
    if (blockIdx.x < 256) role_a(x, wq, ws, smem, blockIdx.x, w, lane);
    else role_g(x, wk, wv, wo, wg, wu, wd, ws, out, smem, blockIdx.x - 256, w, lane);
}

extern "C" void kernel_launch(void* const* d_in, const int* in_sizes, int n_in, void* d_out, int out_size, void* d_ws,
                              size_t ws_size, hipStream_t stream) {
    fused_block<<<512, 256, 0, stream>>>((const u16*)d_in[0], (const u16*)d_in[1], (const u16*)d_in[2], (const u16*)d_in[3],
                                         (const u16*)d_in[4], (const u16*)d_in[6], (const u16*)d_in[7], (const u16*)d_in[8],
                                         (char*)d_ws, (u16*)d_out);
}
```
